```python
import torch

def _softmax(x, axis=-1):
    return torch.softmax(x, dim=axis)
def _gelu(x):
    return torch.nn.functional.gelu(x, approximate="tanh")
def _silu(x):
    return torch.nn.functional.silu(x)
def _sigmoid(x):
    return torch.sigmoid(x)
def _layernorm(x, axis=-1, eps=1e-5):
    m = x.mean(dim=axis, keepdim=True)
    v = x.var(dim=axis, keepdim=True, unbiased=False)
    return (x - m) / torch.sqrt(v + eps)
def _scatter_add(tgt, idx, src, axis=0):
    return tgt.clone().index_add_(axis, idx, src)
def _segment_sum(src, seg, n):
    out = torch.zeros((n,) + src.shape[1:], dtype=src.dtype)
    return out.index_add_(0, seg, src)
def _pad(x, pw):
    flat = tuple(v for pair in reversed(pw) for v in pair)
    return torch.nn.functional.pad(x, flat)

kernel_name = 'recurrent_scan__grouped_kv_mqa__moe_arch_mod__prompt'

def setup_inputs():

    _w = (lambda a: (lambda t: t.to(torch.bfloat16) if t.is_floating_point() else t)(torch.as_tensor(a)))
    def _A_setup():
        B, S, D, H = 1, 2048, 640, 32
        Dh = D // H
        return {
            "x": _w((lambda *s: torch.randn(*s, dtype=torch.bfloat16) * 0.05)(B, S, D)),
            "wq": _w((lambda *s: torch.randn(*s, dtype=torch.bfloat16) * 0.05)(D, H * Dh)),
            "wk": _w((lambda *s: torch.randn(*s, dtype=torch.bfloat16) * 0.05)(D, Dh)),
            "wv": _w((lambda *s: torch.randn(*s, dtype=torch.bfloat16) * 0.05)(D, Dh)),
            "wo": _w((lambda *s: torch.randn(*s, dtype=torch.bfloat16) * 0.05)(H * Dh, D)),
        }
    _A_in = _A_setup()

    def _B_setup():
        B, S, D, F, C = 1, 2048, 640, 1728, 1
        return {
            "x": _w((lambda *s: torch.randn(*s, dtype=torch.bfloat16) * 0.05)(B, S, D)),
            "w_r": _w((lambda *s: torch.randn(*s, dtype=torch.bfloat16) * 1.6)(D)),
            "w_gate": _w((lambda *s: torch.randn(*s, dtype=torch.bfloat16) * 0.1)(D, F)),
            "w_up": _w((lambda *s: torch.randn(*s, dtype=torch.bfloat16) * 0.1)(D, F)),
            "w_down": _w((lambda *s: torch.randn(*s, dtype=torch.bfloat16) * 0.1)(F, D)),
        }
    _B_in = _B_setup()

    return {
        "A_x": _A_in["x"],
        "A_wq": _A_in["wq"],
        "A_wk": _A_in["wk"],
        "A_wv": _A_in["wv"],
        "A_wo": _A_in["wo"],
        "B_x": _B_in["x"],
        "B_w_r": _B_in["w_r"],
        "B_w_gate": _B_in["w_gate"],
        "B_w_up": _B_in["w_up"],
        "B_w_down": _B_in["w_down"],
    }

def reference(A_x, A_wq, A_wk, A_wv, A_wo, B_x, B_w_r, B_w_gate, B_w_up, B_w_down):
    def _A_ref(x, wq, wk, wv, wo):
        B, S, D = x.shape
        H = 32
        Dh = D // H
        q = (lambda x, *d: x.permute(*d))((x @ wq).reshape(B, S, H, Dh), 0, 2, 1, 3)
        k = x @ wk
        v = x @ wv
        scores = torch.einsum("bhqd,bkd->bhqk", q, k) * (Dh ** -0.5)
        p = _softmax(scores, axis=-1)
        o = torch.einsum("bhqk,bkd->bhqd", p, v)
        return (lambda x, *d: x.permute(*d))(o, 0, 2, 1, 3).reshape(B, S, H * Dh) @ wo

    def _B_ref(x, w_r, w_gate, w_up, w_down):
        B, S, D = x.shape
        C = 1
        r = x @ w_r
        idx = torch.argsort(-r, axis=-1)[:, :C]
        idx_e = (lambda x, n, axis: torch.repeat_interleave(x, n, dim=axis))(idx.reshape(B, C, 1), D, axis=2)
        x_sel = (lambda x, i, axis: torch.take_along_dim(x, i, dim=axis))(x, idx_e, axis=1)
        g = _silu(x_sel @ w_gate)
        u = x_sel @ w_up
        y = (g * u) @ w_down
        r_sel = _sigmoid((lambda x, i, axis: torch.take_along_dim(x, i, dim=axis))(r, idx, axis=1))
        y = y * r_sel.reshape(B, C, 1)
        b_off = (torch.arange(B) * S).reshape(B, 1)
        flat_idx = (idx + b_off).reshape(B * C)
        zero = (x * 0.0).reshape(B * S, D)
        upd = _scatter_add(zero, flat_idx, y.reshape(B * C, D), axis=0)
        return x + upd.reshape(B, S, D)

    x = A_x
    xn = _layernorm(x, axis=-1)
    s = _A_ref(xn, A_wq, A_wk, A_wv, A_wo)
    g = _silu(_B_ref(xn, B_w_r, B_w_gate, B_w_up, B_w_down))
    return x + s * g
```

```cpp
#include <hip/hip_runtime.h>
#include <hip/hip_bf16.h>
#include <stdint.h>

typedef __attribute__((ext_vector_type(8))) __bf16 bf16x8;
typedef __attribute__((ext_vector_type(2))) __bf16 bf16x2;
typedef __attribute__((ext_vector_type(4))) short s16x4;
typedef __attribute__((ext_vector_type(8))) short s16x8;
typedef __attribute__((ext_vector_type(4))) float f32x4;
typedef __attribute__((ext_vector_type(16))) float f32x16;
typedef __attribute__((ext_vector_type(4))) unsigned u32x4;
typedef __attribute__((ext_vector_type(2))) unsigned u32x2;
typedef __attribute__((ext_vector_type(2))) float f32x2;
#define LAS __attribute__((address_space(3)))
#define GAS __attribute__((address_space(1)))
#define DI __device__ __forceinline__

constexpr int SEQ = 2048, DM = 640, NH = 32, DH = 20, FF = 1728;
constexpr size_t OFF_Q = 0;
constexpr size_t OFF_KV = OFF_Q + (size_t)NH * SEQ * DH * 2;
constexpr size_t OFF_ST = OFF_KV + 64 * 3072;
constexpr size_t OFF_KEY = OFF_ST + SEQ * 8;
constexpr size_t OFF_IDX = OFF_KEY + 512;
constexpr size_t OFF_O = OFF_IDX + 512;
constexpr size_t OFF_Y = OFF_O + (size_t)SEQ * DM * 2;
constexpr int NYP = FF / 8;

constexpr float QSCALE = 0.22360679774997896f * 1.4426950408889634f;

DI float bf2f(unsigned short b) { return __uint_as_float((unsigned)b << 16); }
DI float rbf(float x) { return (float)(__bf16)x; }
DI unsigned pk2(float lo, float hi) {
  bf16x2 v; v[0] = (__bf16)lo; v[1] = (__bf16)hi; return __builtin_bit_cast(unsigned, v);
}
DI float lo16(unsigned u) { return __uint_as_float(u << 16); }
DI float hi16(unsigned u) { return __uint_as_float(u & 0xffff0000u); }
DI float silu_f(float v) { return v * __builtin_amdgcn_rcpf(1.0f + __builtin_amdgcn_exp2f(-1.4426950408889634f * v)); }

DI void glds16(const void* g, unsigned lds_base) {
  unsigned sv;
  asm volatile("s_mov_b32 %0, m0\n\ts_mov_b32 m0, %2\n\ts_nop 0\n\tglobal_load_lds_dwordx4 %1, off\n\ts_mov_b32 m0, %0"
               : "=&s"(sv) : "v"(g), "s"(lds_base) : "memory");
}
DI void aload16(u32x4& d, const void* g) { asm volatile("global_load_dwordx4 %0, %1, off" : "=v"(d) : "v"(g) : "memory"); }
DI void aload8(u32x2& d, const void* g) { asm volatile("global_load_dwordx2 %0, %1, off" : "=v"(d) : "v"(g) : "memory"); }
DI s16x4 trread(unsigned a) {
  return __builtin_bit_cast(s16x4, __builtin_amdgcn_ds_read_tr16_b64_v4i16((LAS s16x4*)(uintptr_t)a));
}
DI bf16x8 cat8(s16x4 a, s16x4 b) { return __builtin_bit_cast(bf16x8, __builtin_shufflevector(a, b, 0, 1, 2, 3, 4, 5, 6, 7)); }
#define MFMA16(a, b, c) __builtin_amdgcn_mfma_f32_16x16x32_bf16((a), (b), (c), 0, 0, 0)
#define MFMA32(a, b, c) __builtin_amdgcn_mfma_f32_32x32x16_bf16((a), (b), (c), 0, 0, 0)
#define MFMA32K8(a, b, c) __builtin_amdgcn_mfma_f32_32x32x8bf16_1k((a), (b), (c), 0, 0, 0)
DI float dot2(unsigned a, unsigned b, float c) {
  return __builtin_amdgcn_fdot2_f32_bf16(__builtin_bit_cast(bf16x2, a), __builtin_bit_cast(bf16x2, b), c, false);
}
DI int perm16(int p) { return (p & ~12) | ((p & 4) << 1) | ((p & 8) >> 1); }

constexpr int CH = 24576;

constexpr int K1_X = 5 * CH, K1_XS = K1_X + 8 * 3072, K1_XC = K1_XS + 2048, K1_LDS = K1_XC + 1024;

__global__ __launch_bounds__(512) void k1_proj(const char* __restrict__ x, const char* __restrict__ wq, const char* __restrict__ wk,
                                               const char* __restrict__ wv, const char* __restrict__ wr, char* __restrict__ ws) {
  extern __shared__ __attribute__((aligned(16))) char lds[];
  const int tid = threadIdx.x, lane = tid & 63, w = __builtin_amdgcn_readfirstlane(tid >> 6);
  const int b = blockIdx.x, T = (b & 7) * 4 + ((b >> 3) & 3), g = b >> 5;
  const int tb = w & 3, kh = w >> 2, i = lane & 15, g4 = lane >> 4;
  const int eb = g & 3, eh = g >> 2;
  const unsigned lds0 = (unsigned)(uintptr_t)lds;
  const bool do_r = (g == 0);
  const bool has_ext = ((tb >> 1) == eh);

  u32x4 wrf[10];
  if (do_r) {
    const char* wp = wr + (kh * 320 + 8 * g4) * 2;
#pragma unroll
    for (int s = 0; s < 10; ++s) aload16(wrf[s], wp + s * 64);
  }
  const char* src[3]; int sstride[3];
#pragma unroll
  for (int j = 0; j < 3; ++j) {
    const int e = w + 8 * j, slot = e * 64 + lane;
    if (e < 20) {
      const int half = slot / 640, rem = slot % 640, pos = rem / 10, part = rem % 10;
      src[j] = wq + (size_t)(half * 320 + perm16(pos)) * 1280 + g * 160 + part * 16; sstride[j] = 64 * 1280;
    } else {
      const int sl = slot - 1280, half = sl >> 7, pos = (sl & 127) >> 1, part = sl & 1;
      src[j] = ((eb < 2) ? wk : wv) + (half * 320 + perm16(pos)) * 40 + ((eb & 1) ? 24 : part * 16); sstride[j] = 64 * 40;
    }
  }
  const char* xp = x + (size_t)(T * 64 + tb * 16 + i) * 1280 + (kh * 320 + 8 * g4) * 2;
  u32x4 xf[10];
#pragma unroll
  for (int c = 0; c < 5; ++c) {
    aload16(xf[2 * c], xp + (2 * c) * 64);
    aload16(xf[2 * c + 1], xp + (2 * c + 1) * 64);
#pragma unroll
    for (int j = 0; j < 3; ++j) glds16(src[j] + c * sstride[j], lds0 + c * CH + (w + 8 * j) * 1024);
  }

  const int q = i >> 2, p = i & 3, rowp = 16 * (g4 >> 1) + 4 * (g4 & 1) + q;
  const unsigned abase = lds0 + kh * 10240 + rowp * 160 + p * 8;
  const unsigned bbase = lds0 + 20480 + kh * 2048 + rowp * 32 + p * 8;
  f32x4 acc[6], cs0, cs1;
#pragma unroll
  for (int n = 0; n < 6; ++n) acc[n] = (f32x4){0.f, 0.f, 0.f, 0.f};
  cs0 = acc[0]; cs1 = acc[0];
  float sx = 0.f, sxx = 0.f, rr = 0.f, swr = 0.f;
  const unsigned ONES2 = 0x3F803F80u;
  const bf16x8 ones8 = __builtin_bit_cast(bf16x8, (u32x4){ONES2, ONES2, ONES2, ONES2});

#define K1_WAIT(N, c) asm volatile("s_waitcnt vmcnt(" #N ")\n\ts_barrier" : "+v"(xf[2 * c]), "+v"(xf[2 * c + 1]) :: "memory")
#pragma unroll
  for (int c = 0; c < 5; ++c) {
    if (c == 0) {
      K1_WAIT(20, 0);
      if (do_r) {
#pragma unroll
        for (int s = 0; s < 10; ++s) asm volatile("" : "+v"(wrf[s]));
      }
    } else if (c == 1) K1_WAIT(15, 1); else if (c == 2) K1_WAIT(10, 2); else if (c == 3) K1_WAIT(5, 3); else K1_WAIT(0, 4);
#pragma unroll
    for (int sp = 0; sp < 2; ++sp) {
      const int s = 2 * c + sp;
      const bf16x8 xb = __builtin_bit_cast(bf16x8, xf[s]);
      bf16x8 wf[6];
#pragma unroll
      for (int n = 0; n < 5; ++n) {
        const unsigned a = abase + c * CH + sp * 32 * 160 + n * 32;
        wf[n] = cat8(trread(a), trread(a + 8 * 160));
      }
      { const unsigned a = bbase + c * CH + sp * 32 * 32; wf[5] = cat8(trread(a), trread(a + 256)); }
#pragma unroll
      for (int n = 0; n < 5; ++n) acc[n] = MFMA16(wf[n], xb, acc[n]);
      if (has_ext) {
        if (eb < 2) acc[5] = MFMA16(wf[5], xb, acc[5]); else acc[5] = MFMA16(xb, wf[5], acc[5]);
      }
      if (tb == 0) { cs0 = MFMA16(wf[0], ones8, cs0); cs1 = MFMA16(wf[4], ones8, cs1); }
      else if (tb == 1) { cs0 = MFMA16(wf[1], ones8, cs0); cs1 = MFMA16(wf[5], ones8, cs1); }
      else if (tb == 2) cs0 = MFMA16(wf[2], ones8, cs0);
      else cs0 = MFMA16(wf[3], ones8, cs0);
#pragma unroll
      for (int d = 0; d < 4; ++d) { sx = dot2(xf[s][d], ONES2, sx); sxx = dot2(xf[s][d], xf[s][d], sxx); }
      if (do_r) {
#pragma unroll
        for (int d = 0; d < 4; ++d) { rr = dot2(xf[s][d], wrf[s][d], rr); swr = dot2(wrf[s][d], ONES2, swr); }
      }
    }
  }
  sx += __shfl_xor(sx, 16); sxx += __shfl_xor(sxx, 16); rr += __shfl_xor(rr, 16); swr += __shfl_xor(swr, 16);
  sx += __shfl_xor(sx, 32); sxx += __shfl_xor(sxx, 32); rr += __shfl_xor(rr, 32); swr += __shfl_xor(swr, 32);
  {
    f32x4* xw = (f32x4*)(lds + K1_X + w * 3072) + lane;
    if (kh == 0) { xw[0] = acc[3]; xw[64] = acc[4]; xw[128] = acc[5]; }
    else { xw[0] = acc[0]; xw[64] = acc[1]; xw[128] = acc[2]; }
    if (g4 == 0) *(f32x4*)(lds + K1_XS + (kh * 64 + tb * 16 + i) * 16) = (f32x4){sx, sxx, rr, swr};
    if (i == 0) {
      *(f32x4*)(lds + K1_XC + ((kh * 6 + tb) * 16 + 4 * g4) * 4) = cs0;
      if (tb < 2) *(f32x4*)(lds + K1_XC + ((kh * 6 + tb + 4) * 16 + 4 * g4) * 4) = cs1;
    }
  }
  __syncthreads();
  const f32x4* xr = (const f32x4*)(lds + K1_X + (w ^ 4) * 3072) + lane;
  f32x4 o3[3];
#pragma unroll
  for (int n = 0; n < 3; ++n) o3[n] = (kh == 0 ? acc[n] : acc[n + 3]) + xr[64 * n];

  auto stats = [&](int tokl, float& mb, float& db, float& rraw, float& sw) {
    const f32x4 a = *(const f32x4*)(lds + K1_XS + tokl * 16), c2 = *(const f32x4*)(lds + K1_XS + (64 + tokl) * 16);
    const float mean = (a[0] + c2[0]) * (1.0f / DM);
    float var = (a[1] + c2[1]) * (1.0f / DM) - mean * mean; var = fmaxf(var, 0.f);
    mb = rbf(mean);
    db = rbf(__builtin_sqrtf(rbf(rbf(var) + 1e-5f)));
    rraw = a[2] + c2[2]; sw = a[3] + c2[3];
  };
  float mb, db, rraw, sw;
  stats(tb * 16 + i, mb, db, rraw, sw);
  const float rinv = 1.0f / db;
  const int tok = T * 64 + tb * 16 + i;
  auto colsum4 = [&](int blk) {
    return *(const f32x4*)(lds + K1_XC + (blk * 16 + 4 * g4) * 4) + *(const f32x4*)(lds + K1_XC + ((6 + blk) * 16 + 4 * g4) * 4);
  };
#pragma unroll
  for (int n = 0; n < 3; ++n) {
    const int blk = kh * 3 + n;
    if (blk < 5) {
      const f32x4 cs = colsum4(blk);
      const float f = rinv * QSCALE;
      const f32x4 v = (o3[n] - mb * cs) * f;
      const int c4 = 80 * g + 16 * blk + 4 * g4, head = c4 / 20, dim = c4 % 20;
      *(u32x2*)(ws + OFF_Q + ((size_t)(head * SEQ + tok) * 20 + dim) * 2) = (u32x2){pk2(v[0], v[1]), pk2(v[2], v[3])};
    } else if (has_ext) {
      char* img = ws + OFF_KV + (size_t)(T * 2 + (tb >> 1)) * 3072;
      if (eb < 2) {
        const f32x4 cs = colsum4(5);
        const f32x4 v = (o3[n] - mb * cs) * rinv;
        const u32x2 pv = (u32x2){pk2(v[0], v[1]), pk2(v[2], v[3])};
        const int r = (tb & 1) * 16 + i;
        if (eb == 0) *(u32x2*)(img + ((g4 >> 1) * 32 + r) * 16 + (g4 & 1) * 8) = pv;
        else if (g4 == 1) *(u32x2*)(img + 1024 + r * 8) = pv;
        else if (g4 == 2) *(u32x2*)(img + 1024 + 256 + r * 8) = (u32x2){0x00003F80u, 0u};
      } else {
        const float cs = *(const float*)(lds + K1_XC + (5 * 16 + i) * 4) + *(const float*)(lds + K1_XC + (11 * 16 + i) * 4);
        float v[4];
#pragma unroll
        for (int r = 0; r < 4; ++r) {
          float m2, d2, t0, t1; stats(tb * 16 + 4 * g4 + r, m2, d2, t0, t1);
          v[r] = (o3[n][r] - m2 * cs) * (1.0f / d2);
        }
        u32x2 pv = (u32x2){pk2(v[0], v[1]), pk2(v[2], v[3])};
        int d = i;
        bool st = true;
        if (eb == 3) {
          d = 12 + i; st = (i >= 4 && i < 12);
          if (i == 8) pv = (u32x2){ONES2, ONES2}; else if (i > 8) pv = (u32x2){0u, 0u};
        }
        if (st) *(u32x2*)(img + 1536 + (tb & 1) * 768 + ((g4 & 1) * 24 + d) * 16 + (g4 >> 1) * 8) = pv;
      }
    }
  }
  if (do_r && kh == 0) {
    const float r = rbf((rraw - mb * sw) * rinv);
    if (g4 == 0) *(f32x2*)(ws + OFF_ST + (size_t)tok * 8) = (f32x2){mb, db};
    unsigned u = __float_as_uint(r) >> 16;
    u = (u & 0x8000u) ? (~u & 0xffffu) : (u | 0x8000u);
    unsigned key = (u << 16) | (unsigned)(SEQ - 1 - tok);
#pragma unroll
    for (int o = 1; o < 16; o <<= 1) key = max(key, (unsigned)__shfl_xor((int)key, o));
    if (lane == 0) *(unsigned*)(ws + OFF_KEY + (T * 4 + tb) * 4) = key;
  }
}

constexpr int K2_SLOTS = 4, K2_H = K2_SLOTS * CH, K2_LDS = K2_H + 1024;

DI float wave_sum32(float v) {
#pragma unroll
  for (int o = 1; o < 32; o <<= 1) v += __shfl_xor(v, o);
  return v;
}

__global__ __launch_bounds__(768) void k2_attn(const char* __restrict__ x, const char* __restrict__ wg, const char* __restrict__ wu,
                                               const char* __restrict__ wd, char* __restrict__ ws) {
  extern __shared__ __attribute__((aligned(16))) char lds[];
  const int tid = threadIdx.x, lane = tid & 63, w = __builtin_amdgcn_readfirstlane(tid >> 6);
  const int b = blockIdx.x, T = (b & 7) * 4 + ((b >> 3) & 3), g = b >> 5;
  const unsigned lds0 = (unsigned)(uintptr_t)lds;
  const int r = lane & 31, h = lane >> 5;

  if (w >= 8) {
    const int hw = w - 8;
    const int cg = (b & 7) * 27 + (b >> 3);
    if ((b >> 3) >= 27) return;
    const unsigned* keys = (const unsigned*)(ws + OFF_KEY);
    const unsigned key0 = keys[lane], key1 = keys[64 + lane];
    u32x4 wv[5]; u32x2 dv[8];
    {
      const char* wm = (h ? wu : wg) + (size_t)(160 * hw + r) * (FF * 2) + cg * 16;
#pragma unroll
      for (int j = 0; j < 5; ++j) wv[j] = *(const u32x4*)(wm + (size_t)j * 32 * (FF * 2));
      const char* dp = wd + (size_t)(cg * 8) * 1280 + (160 * hw + 4 * min(lane, 39)) * 2;
#pragma unroll
      for (int j = 0; j < 8; ++j) dv[j] = *(const u32x2*)(dp + j * 1280);
    }
    asm volatile("s_barrier" ::: "memory");
    unsigned key = max(key0, key1);
#pragma unroll
    for (int o = 1; o < 64; o <<= 1) key = max(key, (unsigned)__shfl_xor((int)key, o));
    const int idx = SEQ - 1 - (int)(key & 0xffffu);
    if (b == 0 && hw == 0 && lane == 0) {
      unsigned u = key >> 16; u = (u & 0x8000u) ? (u & 0x7fffu) : (~u & 0xffffu);
      const float rv = bf2f((unsigned short)u);
      const float sg = rbf(__builtin_amdgcn_rcpf(1.0f + __builtin_amdgcn_exp2f(-1.4426950408889634f * rv)));
      *(int*)(ws + OFF_IDX) = idx; *(float*)(ws + OFF_IDX + 4) = sg;
    }
    const f32x2 st = *(const f32x2*)(ws + OFF_ST + (size_t)idx * 8);
    unsigned short xs[5];
#pragma unroll
    for (int j = 0; j < 5; ++j) xs[j] = *(const unsigned short*)(x + (size_t)idx * 1280 + (160 * hw + 32 * j + r) * 2);
    asm volatile("s_barrier" ::: "memory");
    float a8[8];
#pragma unroll
    for (int c = 0; c < 8; ++c) a8[c] = 0.f;
    const float ri = 1.0f / st[1];
#pragma unroll
    for (int j = 0; j < 5; ++j) {
      const float xn = rbf(rbf(bf2f(xs[j]) - st[0]) * ri);
#pragma unroll
      for (int c = 0; c < 4; ++c) { a8[2 * c] += xn * lo16(wv[j][c]); a8[2 * c + 1] += xn * hi16(wv[j][c]); }
    }
#pragma unroll
    for (int c = 0; c < 8; ++c) a8[c] = wave_sum32(a8[c]);
    float* red = (float*)(lds + K2_H);
    if (r == 0) {
      *(f32x4*)(red + (hw * 2 + h) * 8) = (f32x4){a8[0], a8[1], a8[2], a8[3]};
      *(f32x4*)(red + (hw * 2 + h) * 8 + 4) = (f32x4){a8[4], a8[5], a8[6], a8[7]};
    }
    asm volatile("s_waitcnt lgkmcnt(0)\n\ts_barrier" ::: "memory");
    const int c8 = lane & 7;
    const float gp = rbf(red[c8] + red[16 + c8] + red[32 + c8] + red[48 + c8]);
    const float up = rbf(red[8 + c8] + red[24 + c8] + red[40 + c8] + red[56 + c8]);
    const float hv = rbf(rbf(silu_f(gp)) * up);
    f32x4 y = (f32x4){0.f, 0.f, 0.f, 0.f};
#pragma unroll
    for (int j = 0; j < 8; ++j) {
      const float hj = __uint_as_float(__builtin_amdgcn_readlane(__float_as_uint(hv), j));
      y[0] += hj * lo16(dv[j][0]); y[1] += hj * hi16(dv[j][0]); y[2] += hj * lo16(dv[j][1]); y[3] += hj * hi16(dv[j][1]);
    }
    if (lane < 40) *(f32x4*)(ws + OFF_Y + ((size_t)cg * DM + 160 * hw + 4 * lane) * 4) = y;
    return;
  }

  const int head = 4 * g + (w >> 1), tok = T * 64 + (w & 1) * 32 + r;
  const char* kvg = ws + OFF_KV + w * 3072 + lane * 16;
  auto dma = [&](int c) {
    const unsigned dst = lds0 + (c & (K2_SLOTS - 1)) * CH + w * 3072;
#pragma unroll
    for (int j = 0; j < 3; ++j) glds16(kvg + (size_t)c * CH + j * 1024, dst + j * 1024);
  };
  dma(0);
  const char* qp = ws + OFF_Q + (size_t)(head * SEQ + tok) * 40;
  u32x2 qa = *(const u32x2*)(qp + h * 16), qb = *(const u32x2*)(qp + h * 16 + 8);
  u32x2 qx = *(const u32x2*)(qp + 32);
  asm volatile("s_waitcnt vmcnt(0)\n\ts_barrier" : "+v"(qa), "+v"(qb), "+v"(qx) :: "memory");
  const bf16x8 qf = __builtin_bit_cast(bf16x8, (u32x4){qa[0], qa[1], qb[0], qb[1]});
  if (h) qx = (u32x2){0u, 0u};
  dma(1); dma(2);

  const int dd = r < 24 ? r : (r < 28 ? r - 4 : r - 12);
  const unsigned koff = lane * 16, xoff = 1024 + lane * 8, voff = 1536 + (h * 24 + dd) * 16;
  const LAS char* L = (const LAS char*)(uintptr_t)lds0;
  f32x16 zero16;
#pragma unroll
  for (int n = 0; n < 16; ++n) zero16[n] = 0.f;
#define LDK(base, t) (*(const LAS bf16x8*)(L + (base) + (t) * 3072 + koff))
#define LDX(base, t) (*(const LAS s16x4*)(L + (base) + (t) * 3072 + xoff))
#define LDV(base, t, s) (*(const LAS bf16x8*)(L + (base) + (t) * 3072 + (s) * 768 + voff))
  {
    f32x16 s0 = MFMA32(LDK(0, 0), qf, zero16);
    s0 = MFMA32K8(LDX(0, 0), __builtin_bit_cast(s16x4, qx), s0);
    float m = s0[0];
#pragma unroll
    for (int n = 1; n < 16; ++n) m = fmaxf(m, s0[n]);
    m = fmaxf(m, __shfl_xor(m, 32));
    if (h) qx[0] = pk2(-m, 0.f);
  }
  const s16x4 qxf = __builtin_bit_cast(s16x4, qx);
  f32x16 o = zero16;
  f32x16 sa = MFMA32K8(LDX(0, 0), qxf, MFMA32(LDK(0, 0), qf, zero16)), sb;
#define SOFTPV(S, base, t)                                                                                         \
  {                                                                                                                \
    u32x4 p0, p1;                                                                                                  \
    _Pragma("unroll") for (int n = 0; n < 4; ++n)                                                                  \
      p0[n] = pk2(__builtin_amdgcn_exp2f(S[2 * n]), __builtin_amdgcn_exp2f(S[2 * n + 1]));                         \
    o = MFMA32(LDV(base, t, 0), __builtin_bit_cast(bf16x8, p0), o);                                                \
    _Pragma("unroll") for (int n = 0; n < 4; ++n)                                                                  \
      p1[n] = pk2(__builtin_amdgcn_exp2f(S[8 + 2 * n]), __builtin_amdgcn_exp2f(S[8 + 2 * n + 1]));                 \
    o = MFMA32(LDV(base, t, 1), __builtin_bit_cast(bf16x8, p1), o);                                                \
  }
#define QK(base, t) MFMA32K8(LDX(base, t), qxf, MFMA32(LDK(base, t), qf, zero16))
  for (int c = 0; c < 8; ++c) {
    const unsigned cur = (c & (K2_SLOTS - 1)) * CH;
    const unsigned nxt = (c == 7) ? cur : ((c + 1) & (K2_SLOTS - 1)) * CH;
    sb = QK(cur, 1); SOFTPV(sa, cur, 0);
    sa = QK(cur, 2); SOFTPV(sb, cur, 1);
    sb = QK(cur, 3); SOFTPV(sa, cur, 2);
    sa = QK(cur, 4); SOFTPV(sb, cur, 3);
    sb = QK(cur, 5); SOFTPV(sa, cur, 4);
    sa = QK(cur, 6); SOFTPV(sb, cur, 5);
    sb = QK(cur, 7); SOFTPV(sa, cur, 6);
    if (c < 6) asm volatile("s_waitcnt vmcnt(3)\n\ts_barrier" ::: "memory");
    else asm volatile("s_waitcnt vmcnt(0)\n\ts_barrier" ::: "memory");
    if (c < 5) dma(c + 3);
    sa = QK(nxt, 0); SOFTPV(sb, cur, 7);
  }
  float l = __shfl(o[8], r + 32);
  {
    bool ok = (l >= 8.6736174e-19f) && (l <= 1.1529215e18f);
#pragma unroll
    for (int n = 0; n < 12; ++n) ok = ok && (fabsf(o[n]) <= 3.0e38f);
    if (!__all(ok)) {
      const char* kg = ws + OFF_KV;
      const s16x4 qx0 = __builtin_bit_cast(s16x4, h ? (u32x2){0u, 0u} : qx);
      float m = -INFINITY;
      for (int t = 0; t < 64; ++t) {
        f32x16 s = MFMA32(*(const bf16x8*)(kg + t * 3072 + koff), qf, zero16);
        s = MFMA32K8(*(const s16x4*)(kg + t * 3072 + xoff), qx0, s);
#pragma unroll
        for (int n = 0; n < 16; ++n) m = fmaxf(m, s[n]);
      }
      m = fmaxf(m, __shfl_xor(m, 32));
      o = zero16;
      for (int t = 0; t < 64; ++t) {
        f32x16 s = MFMA32(*(const bf16x8*)(kg + t * 3072 + koff), qf, zero16);
        s = MFMA32K8(*(const s16x4*)(kg + t * 3072 + xoff), qx0, s);
        u32x4 p0, p1;
#pragma unroll
        for (int n = 0; n < 4; ++n) {
          p0[n] = pk2(__builtin_amdgcn_exp2f(s[2 * n] - m), __builtin_amdgcn_exp2f(s[2 * n + 1] - m));
          p1[n] = pk2(__builtin_amdgcn_exp2f(s[8 + 2 * n] - m), __builtin_amdgcn_exp2f(s[8 + 2 * n + 1] - m));
        }
        o = MFMA32(*(const bf16x8*)(kg + t * 3072 + voff), __builtin_bit_cast(bf16x8, p0), o);
        o = MFMA32(*(const bf16x8*)(kg + t * 3072 + 768 + voff), __builtin_bit_cast(bf16x8, p1), o);
      }
      l = __shfl(o[8], r + 32);
    }
  }
  const float li = 1.0f / l;
  char* op = ws + OFF_O + (size_t)tok * 1280 + head * 40 + h * 8;
  *(u32x2*)(op) = (u32x2){pk2(o[0] * li, o[1] * li), pk2(o[2] * li, o[3] * li)};
  *(u32x2*)(op + 16) = (u32x2){pk2(o[4] * li, o[5] * li), pk2(o[6] * li, o[7] * li)};
  if (!h) *(u32x2*)(op + 32) = (u32x2){pk2(o[8] * li, o[9] * li), pk2(o[10] * li, o[11] * li)};
}

constexpr int K3_CH = 20480, K3_X = 5 * K3_CH, K3_Y = K3_X + 8 * 3072, K3_LDS = K3_Y + 25 * 320;

__global__ __launch_bounds__(512) void k3_out(const char* __restrict__ x, const char* __restrict__ wo, const char* __restrict__ ws,
                                              char* __restrict__ out) {
  extern __shared__ __attribute__((aligned(16))) char lds[];
  const int tid = threadIdx.x, lane = tid & 63, w = __builtin_amdgcn_readfirstlane(tid >> 6);
  const int b = blockIdx.x, T = (b & 7) * 4 + ((b >> 3) & 3), g = b >> 5;
  const int tb = w & 3, kh = w >> 2, i = lane & 15, g4 = lane >> 4;
  const unsigned lds0 = (unsigned)(uintptr_t)lds;
  const int idx = *(const int*)(ws + OFF_IDX);
  const float rsel = *(const float*)(ws + OFF_IDX + 4);

  const char* src[3];
#pragma unroll
  for (int j = 0; j < 3; ++j) {
    const int e = min(w + 8 * j, 19), slot = e * 64 + lane;
    const int half = slot / 640, rem = slot % 640, pos = rem / 10, part = rem % 10;
    src[j] = wo + (size_t)(half * 320 + perm16(pos)) * 1280 + g * 160 + part * 16;
  }
  const int tok = T * 64 + tb * 16 + i;
  u32x2 stu, xe[3];
  aload8(stu, ws + OFF_ST + (size_t)tok * 8);
  const size_t eoff = (size_t)tok * 1280 + (80 * g + 48 * kh + 4 * g4) * 2;
#pragma unroll
  for (int n = 0; n < 3; ++n) aload8(xe[n], x + eoff + ((kh == 1 && n == 2) ? 0 : n * 32));
  const char* op = ws + OFF_O + (size_t)tok * 1280 + (kh * 320 + 8 * g4) * 2;
  u32x4 xf[10];
#pragma unroll
  for (int c = 0; c < 5; ++c) {
    aload16(xf[2 * c], op + (2 * c) * 64);
    aload16(xf[2 * c + 1], op + (2 * c + 1) * 64);
#pragma unroll
    for (int j = 0; j < 3; ++j) glds16(src[j] + c * 64 * 1280, lds0 + c * K3_CH + min(w + 8 * j, 19) * 1024);
  }
  const bool sel = (idx >> 6) == T;
  u32x4 yv[9];
  const int rg = min(tid / 20, 24), c4 = tid % 20;
  if (sel) {
    const char* yp = ws + OFF_Y + ((size_t)80 * g + 4 * c4) * 4;
#pragma unroll
    for (int j = 0; j < 9; ++j) aload16(yv[j], yp + (size_t)min(rg + 25 * j, NYP - 1) * DM * 4);
  }

  const int q = i >> 2, p = i & 3, rowp = 16 * (g4 >> 1) + 4 * (g4 & 1) + q;
  const unsigned abase = lds0 + kh * 10240 + rowp * 160 + p * 8;
  f32x4 acc[5];
#pragma unroll
  for (int n = 0; n < 5; ++n) acc[n] = (f32x4){0.f, 0.f, 0.f, 0.f};
#pragma unroll
  for (int c = 0; c < 5; ++c) {
    if (sel) {
      if (c == 0) K1_WAIT(29, 0); else if (c == 1) K1_WAIT(24, 1); else if (c == 2) K1_WAIT(19, 2); else if (c == 3) K1_WAIT(14, 3); else K1_WAIT(9, 4);
    } else {
      if (c == 0) K1_WAIT(20, 0); else if (c == 1) K1_WAIT(15, 1); else if (c == 2) K1_WAIT(10, 2); else if (c == 3) K1_WAIT(5, 3); else K1_WAIT(0, 4);
    }
#pragma unroll
    for (int sp = 0; sp < 2; ++sp) {
      const bf16x8 xb = __builtin_bit_cast(bf16x8, xf[2 * c + sp]);
#pragma unroll
      for (int n = 0; n < 5; ++n) {
        const unsigned a = abase + c * K3_CH + sp * 32 * 160 + n * 32;
        acc[n] = MFMA16(cat8(trread(a), trread(a + 8 * 160)), xb, acc[n]);
      }
    }
  }
  {
    f32x4* xw = (f32x4*)(lds + K3_X + w * 3072) + lane;
    if (kh == 0) { xw[0] = acc[3]; xw[64] = acc[4]; }
    else { xw[0] = acc[0]; xw[64] = acc[1]; xw[128] = acc[2]; }
    if (sel) {
      asm volatile("s_waitcnt vmcnt(0)" : "+v"(yv[0]), "+v"(yv[1]), "+v"(yv[2]), "+v"(yv[3]), "+v"(yv[4]), "+v"(yv[5]), "+v"(yv[6]),
                   "+v"(yv[7]), "+v"(yv[8]) :: "memory");
      f32x4 ysum = __builtin_bit_cast(f32x4, yv[0]);
#pragma unroll
      for (int j = 1; j < 9; ++j)
        if (rg + 25 * j < NYP) ysum += __builtin_bit_cast(f32x4, yv[j]);
      if (tid < 500) *(f32x4*)(lds + K3_Y + rg * 320 + c4 * 16) = ysum;
    }
  }
  __syncthreads();
  asm volatile("" : "+v"(stu), "+v"(xe[0]), "+v"(xe[1]), "+v"(xe[2]));
  const f32x2 st = __builtin_bit_cast(f32x2, stu);
  const f32x4* xr = (const f32x4*)(lds + K3_X + (w ^ 4) * 3072) + lane;
  const float ri = 1.0f / st[1];
#pragma unroll
  for (int n = 0; n < 3; ++n) {
    if (kh == 1 && n == 2) break;
    const f32x4 s = (kh == 0 ? acc[n] : acc[n + 3]) + xr[64 * n];
    float xv[4] = {lo16(xe[n][0]), hi16(xe[n][0]), lo16(xe[n][1]), hi16(xe[n][1])};
    float z[4];
#pragma unroll
    for (int e = 0; e < 4; ++e) z[e] = rbf(rbf(xv[e] - st[0]) * ri);
    if (tok == idx) {
      f32x4 y = (f32x4){0.f, 0.f, 0.f, 0.f};
#pragma unroll 5
      for (int j = 0; j < 25; ++j) y += *(const f32x4*)(lds + K3_Y + j * 320 + ((kh * 3 + n) * 4 + g4) * 16);
#pragma unroll
      for (int e = 0; e < 4; ++e) z[e] = rbf(z[e] + rbf(rbf(y[e]) * rsel));
    }
    float ov[4];
#pragma unroll
    for (int e = 0; e < 4; ++e) ov[e] = xv[e] + rbf(rbf(s[e]) * rbf(silu_f(z[e])));
    *(u32x2*)(out + eoff + n * 32) = (u32x2){pk2(ov[0], ov[1]), pk2(ov[2], ov[3])};
  }
}

extern "C" void kernel_launch(void* const* d_in, const int* in_sizes, int n_in, void* d_out, int out_size, void* d_ws,
                              size_t ws_size, hipStream_t stream) {
  const char* x = (const char*)d_in[0];
  const char* wq = (const char*)d_in[1];
  const char* wk = (const char*)d_in[2];
  const char* wv = (const char*)d_in[3];
  const char* wo = (const char*)d_in[4];
  const char* wr = (const char*)d_in[6];
  const char* wg = (const char*)d_in[7];
  const char* wu = (const char*)d_in[8];
  const char* wd = (const char*)d_in[9];
  char* ws = (char*)d_ws;
  (void)hipFuncSetAttribute((const void*)k1_proj, hipFuncAttributeMaxDynamicSharedMemorySize, K1_LDS);
  (void)hipFuncSetAttribute((const void*)k2_attn, hipFuncAttributeMaxDynamicSharedMemorySize, K2_LDS);
  (void)hipFuncSetAttribute((const void*)k3_out, hipFuncAttributeMaxDynamicSharedMemorySize, K3_LDS);
  k1_proj<<<256, 512, K1_LDS, stream>>>(x, wq, wk, wv, wr, ws);
  k2_attn<<<256, 768, K2_LDS, stream>>>(x, wg, wu, wd, ws);
  k3_out<<<256, 512, K3_LDS, stream>>>(x, wo, ws, (char*)d_out);
}
```

```cpp
#include <hip/hip_runtime.h>
#include <hip/hip_bf16.h>
#include <stdint.h>
#include <type_traits>

typedef __attribute__((ext_vector_type(8))) __bf16 bf16x8;
typedef __attribute__((ext_vector_type(2))) __bf16 bf16x2;
typedef __attribute__((ext_vector_type(4))) short s16x4;
typedef __attribute__((ext_vector_type(4))) float f32x4;
typedef __attribute__((ext_vector_type(16))) float f32x16;
typedef __attribute__((ext_vector_type(4))) unsigned u32x4;
typedef __attribute__((ext_vector_type(2))) unsigned u32x2;
typedef __attribute__((ext_vector_type(2))) float f32x2;
typedef unsigned long long u64;
#define LAS __attribute__((address_space(3)))
#define DI __device__ __forceinline__

constexpr int SEQ = 2048, DM = 640, FF = 1728, NYP = FF / 8;
constexpr unsigned OFF_EP = 0;
constexpr unsigned OFF_FKV = 256;
constexpr unsigned OFF_FS = OFF_FKV + 2048;
constexpr unsigned OFF_FY = OFF_FS + 4096;
constexpr unsigned OFF_KEY = OFF_FY + 2048;
constexpr unsigned OFF_ST = OFF_KEY + 512;
constexpr unsigned OFF_KV = OFF_ST + SEQ * 8;
constexpr unsigned OFF_KG = OFF_KV + 64 * 3072;
constexpr unsigned OFF_O = OFF_KG + 64 * 6144;
constexpr unsigned OFF_Y = OFF_O + SEQ * DM * 2;
constexpr unsigned WS_USED = OFF_Y + NYP * DM * 4;

constexpr unsigned SENT = 0xffffffffu;
constexpr float LOG2E = 1.4426950408889634f;
constexpr float QSCALE = 0.22360679774997896f * LOG2E;

constexpr unsigned L_XA = 0, L_XB = 81920, L_S0 = 108544, L_S1 = 129024, L_WR = 149504;
constexpr unsigned L_WO = 0, L_FG = 61440, L_FD = 81920, L_RING = 102400;
constexpr unsigned L_M = 151552;
constexpr unsigned L_STP = L_M;
constexpr unsigned L_CSP = L_M + 512;
constexpr unsigned L_STF = L_M + 2304;
constexpr unsigned L_CSQ = L_M + 2816;
constexpr unsigned L_RDY = L_M + 3136;
constexpr unsigned L_HC = L_M + 3152;
constexpr unsigned L_PRG = L_M + 3168;
constexpr unsigned L_XRD = L_M + 3184;
constexpr unsigned L_IDX = L_M + 3216;
constexpr unsigned L_EP = L_M + 3224;
constexpr unsigned L_EX = L_M + 3328;
constexpr unsigned L_XS = L_M + 3328;
constexpr unsigned L_FP = L_M + 4608;
constexpr unsigned L_YR = L_M + 4864;
constexpr unsigned L_Y2 = L_M + 8704;
constexpr unsigned L_ONE = L_M + 9472;
constexpr unsigned L_KST = L_M + 10752;
constexpr unsigned L_CNT = L_M + 12064;
constexpr unsigned L_FRE = L_M + 12032;
constexpr unsigned LDS_BYTES = 163840;
constexpr int CODE_BYTES = 40960;

DI float bf2f(unsigned short b) { return __uint_as_float((unsigned)b << 16); }
DI float rbf(float x) { return (float)(__bf16)x; }
DI unsigned pk2(float lo, float hi) { bf16x2 v; v[0] = (__bf16)lo; v[1] = (__bf16)hi; return __builtin_bit_cast(unsigned, v); }
DI float lo16(unsigned u) { return __uint_as_float(u << 16); }
DI float hi16(unsigned u) { return __uint_as_float(u & 0xffff0000u); }
DI float sigm(float v) { return __builtin_amdgcn_rcpf(1.0f + __builtin_amdgcn_exp2f(-LOG2E * v)); }

DI void glds16(const void* g, unsigned lds_base) {
  unsigned sv;
  asm volatile("s_mov_b32 %0, m0\n\ts_mov_b32 m0, %2\n\ts_nop 0\n\tglobal_load_lds_dwordx4 %1, off\n\ts_mov_b32 m0, %0"
               : "=&s"(sv) : "v"(g), "s"(lds_base) : "memory");
}
#define GLDS(sbase, voff, lds_base, POL)                                                                                          \
  do { unsigned sv_;                                                                                                               \
    asm volatile("s_mov_b32 %0, m0\n\ts_mov_b32 m0, %3\n\ts_nop 0\n\tglobal_load_lds_dwordx4 %1, %2" POL "\n\ts_mov_b32 m0, %0"  \
                 : "=&s"(sv_) : "v"(voff), "s"(sbase), "s"(lds_base) : "memory"); } while (0)
DI s16x4 trread(unsigned a) { return __builtin_bit_cast(s16x4, __builtin_amdgcn_ds_read_tr16_b64_v4i16((LAS s16x4*)(uintptr_t)a)); }
DI bf16x8 cat8(s16x4 a, s16x4 b) { return __builtin_bit_cast(bf16x8, __builtin_shufflevector(a, b, 0, 1, 2, 3, 4, 5, 6, 7)); }
#define MFMA16(a, b, c) __builtin_amdgcn_mfma_f32_16x16x32_bf16((a), (b), (c), 0, 0, 0)
#define MFMA32(a, b, c) __builtin_amdgcn_mfma_f32_32x32x16_bf16((a), (b), (c), 0, 0, 0)
#define MFMA32K8(a, b, c) __builtin_amdgcn_mfma_f32_32x32x8bf16_1k((a), (b), (c), 0, 0, 0)
#define BC8(x) __builtin_bit_cast(bf16x8, x)
DI int perm16(int p) { return (p & ~12) | ((p & 4) << 1) | ((p & 8) >> 1); }
#define LDSR(T, off) (*(const LAS T*)(uintptr_t)(off))
#define LDSW(T, off) (*(LAS T*)(uintptr_t)(off))
#define LDSV(off) (*(volatile LAS unsigned*)(uintptr_t)(off))
#define BAR() asm volatile("s_waitcnt lgkmcnt(0)\n\ts_barrier" ::: "memory")
#define VWAIT(N) asm volatile("s_waitcnt vmcnt(" #N ")" ::: "memory")
#define SB() __builtin_amdgcn_sched_barrier(0)
#define PIN(x) asm volatile("" : "+v"(x))
#define SC1 16
#define LD128(off) __builtin_amdgcn_raw_buffer_load_b128(rs, (off), 0, SC1)
#define LD64(off) __builtin_amdgcn_raw_buffer_load_b64(rs, (off), 0, SC1)
#define LD32(off) __builtin_amdgcn_raw_buffer_load_b32(rs, (off), 0, SC1)
#define ST128(v, off) __builtin_amdgcn_raw_buffer_store_b128((v), rs, (off), 0, SC1)
#define ST64(v, off) __builtin_amdgcn_raw_buffer_store_b64((v), rs, (off), 0, SC1)
#define ST32(v, off) __builtin_amdgcn_raw_buffer_store_b32((v), rs, (off), 0, SC1)
constexpr unsigned SPIN_CAP = 1u << 16;
#define SPIN(cond, slp) do { _Pragma("nounroll") for (unsigned sp_ = 0; sp_ < SPIN_CAP; ++sp_) { if (cond) break; __builtin_amdgcn_s_sleep(slp); } \
                             asm volatile("" ::: "memory"); } while (0)
#define RFL(x) __builtin_amdgcn_readfirstlane(x)
#define LGKM0() asm volatile("s_waitcnt lgkmcnt(0)" ::: "memory")
#define LDS_INC(a) do { asm volatile("" ::: "memory"); if (lane == 0) __hip_atomic_fetch_add((LAS unsigned*)(uintptr_t)(a), 1u, __ATOMIC_RELAXED, __HIP_MEMORY_SCOPE_WORKGROUP); } while (0)

DI unsigned min4(unsigned a) {
  const u32x4 f = *(volatile LAS u32x4*)(uintptr_t)a;
  return RFL(min(min(f[0], f[1]), min(f[2], f[3])));
}
DI float diag16(const f32x4& d, int i, unsigned src4) {
  const float v = (i & 2) ? ((i & 1) ? d[3] : d[2]) : ((i & 1) ? d[1] : d[0]);
  return __int_as_float(__builtin_amdgcn_ds_bpermute((int)src4, __float_as_int(v)));
}
DI void ln_stats(float sx, float sxx, float& mb, float& db) {
  const float mean = sx * (1.0f / DM);
  const float var = fmaxf(sxx * (1.0f / DM) - mean * mean, 0.f);
  mb = rbf(mean);
  db = rbf(__builtin_amdgcn_sqrtf(rbf(rbf(var) + 1e-5f)));
}

__global__ __launch_bounds__(768, 3) void fused_block(const char* __restrict__ x, const char* __restrict__ wq, const char* __restrict__ wk,
                                                   const char* __restrict__ wv, const char* __restrict__ wo, const char* __restrict__ wr,
                                                   const char* __restrict__ wg, const char* __restrict__ wu, const char* __restrict__ wd,
                                                   char* __restrict__ ws, char* __restrict__ out) {
  const int tid = threadIdx.x, lane = tid & 63, w = RFL(tid >> 6);
  const int b = blockIdx.x, T = (b & 7) * 4 + ((b >> 3) & 3), g = b >> 5;
  const int kt = g >> 2, tbk = g & 3;
  constexpr unsigned L0 = 0;
  const __amdgpu_buffer_rsrc_t rs = __builtin_amdgcn_make_buffer_rsrc(ws, 0, WS_USED, 0x00020000);
  const int i = lane & 15, g4 = lane >> 4;
  const unsigned ONES2 = 0x3F803F80u;
  const f32x4 z4 = (f32x4){0.f, 0.f, 0.f, 0.f};
  const int rowp = 16 * (g4 >> 1) + 4 * (g4 & 1) + (i >> 2);
  const unsigned trc = (i & 3) * 8;

  if (w >= 8) {
    const int hw = w - 8;
    const unsigned prg = L0 + L_PRG + hw * 4, lo16b = lane * 16;
    __builtin_amdgcn_s_setprio(3);
    auto rotc = [&](int j) { const int c = j + (T & 3); return c < 5 ? c : c - 5; };
    {
      const unsigned xo = g4 * 1280 + ((i ^ (4 * hw + g4)) << 4);
      const char* xb = x + (size_t)(T * 64 + 4 * hw) * 1280;
      auto xdma = [&](int rb) {
#pragma unroll
        for (int j = 0; j < 5; ++j) {
          const int pos = (j + 2 * kt < 5) ? j + 2 * kt : j + 2 * kt - 5;
          GLDS(xb + rb * 20480 + rotc(pos) * 256, xo, L0 + L_XA + rb * 20480 + pos * 4096 + hw * 1024, "");
        }
      };
      const char* wext = kt ? wv : wk;
#pragma unroll
      for (int j = 0; j < 5; ++j) GLDS(wext + rotc(j) * 5120 + hw * 1024, lo16b, L0 + L_XB + j * 5120 + hw * 1024, "");
      GLDS(wext + rotc(hw) * 5120 + 4096, lo16b, L0 + L_XB + hw * 5120 + 4096, "");
      if (hw == 0) GLDS(wext + rotc(4) * 5120 + 4096, lo16b, L0 + L_XB + 4 * 5120 + 4096, "");
      if (hw == 1) GLDS(wr, (unsigned)(rotc(g4) * 256 + i * 16), L0 + L_WR, "");
      if (hw == 2) GLDS(wr + rotc(4) * 256, (unsigned)i * 16, L0 + L_WR + 1024, "");
      xdma(tbk);
      BAR();
      VWAIT(5); LDSV(prg) = 1;
      xdma((tbk + 1) & 3);
      VWAIT(5); LDSV(prg) = 2;
      xdma((tbk + 2) & 3);
      VWAIT(5); LDSV(prg) = 3;
      xdma((tbk + 3) & 3);
    }
    unsigned wof[5];
#pragma unroll
    for (int m = 0; m < 5; ++m) { const int slot = m * 64 + lane, pos = slot / 10; wof[m] = perm16(pos) * 1280 + (slot - pos * 10) * 16; }
    auto wdma = [&](const char* wsrc, int c, unsigned dst, int blk) {
      const char* sb_ = wsrc + (size_t)(c * 128 + blk * 32) * 1280 + g * 160;
#pragma unroll
      for (int m = 0; m < 5; ++m) GLDS(sb_, wof[m], L0 + dst + blk * 5120 + m * 1024, "");
    };
    const int qb = hw;
    wdma(wq, rotc(0), L_S0, qb);
    VWAIT(5); LDSV(prg) = 4;
    wdma(wq, rotc(1), L_S1, qb);
    SPIN(RFL(min(LDSV(L0 + L_XRD + tbk * 4), LDSV(L0 + L_XRD + 16 + tbk * 4))) >= 1u, 1);
    wdma(wq, rotc(2), L_XB, qb);
    VWAIT(10); LDSV(prg) = 5;
#pragma unroll
    for (int c = 3; c < 5; ++c) {
      const int blk = (tbk + c - 2) & 3;
      SPIN(RFL(min(LDSV(L0 + L_XRD + blk * 4), LDSV(L0 + L_XRD + 16 + blk * 4))) >= 3u, 1);
      wdma(wq, rotc(c), L_XA + blk * 20480, qb);
      if (c == 3) { VWAIT(10); LDSV(prg) = 6; }
    }
    VWAIT(10); LDSV(prg) = 7;
    VWAIT(5); LDSV(prg) = 8;
    VWAIT(0); LDSV(prg) = 9;
    SPIN(RFL(LDSV(L0 + L_IDX + 4)) != 0u, 1);
    const unsigned tlo = LDSV(L0 + L_EP), thi = LDSV(L0 + L_EP + 4);
    auto kvready = [&]() {
      const u32x4 f = LD128(OFF_FKV + lane * 32), e = LD128(OFF_FKV + lane * 32 + 16);
      const u64 m = __ballot(f[0] == tlo && f[1] == thi && f[2] == tlo && f[3] == thi && e[0] == tlo && e[1] == thi && e[2] == tlo && e[3] == thi);
      return m & (m >> 1) & 0x5555555555555555ull;
    };
    {
      const unsigned src = OFF_KG + ((T & ~3) + hw) * 12288;
      SPIN(RFL(LDSV(L0 + L_HC + 12)) != 0u, 2);
      __builtin_amdgcn_s_sleep(8);
      {
        u32x2 f;
        const unsigned smp = src + ((lane >> 2) & 1) * 6144 + ((lane & 2) ? 3072 + (lane & 1) * 1536 : (lane & 1) * 512);
        SPIN((f = LD64(smp), __all(f[1] == tlo)), 6);
      }
      u32x4 gq[12];
      _Pragma("nounroll") for (unsigned sp_ = 0; sp_ < SPIN_CAP; ++sp_) {
#pragma unroll
        for (int j = 0; j < 12; ++j) gq[j] = LD128(src + j * 1024 + lo16b);
        SB();
        unsigned bad = 0;
#pragma unroll
        for (int j = 0; j < 12; ++j) bad |= (gq[j][1] ^ tlo) | (gq[j][3] ^ tlo);
        if (__all(bad == 0u)) break;
        __builtin_amdgcn_s_sleep(4);
      }
      SPIN(RFL(LDSV(L0 + L_CNT)) >= 8u, 1);
#pragma unroll
      for (int j = 0; j < 12; ++j)
        LDSW(u32x2, L0 + L_RING + (hw & 1) * 24576 + (hw >> 1) * 6144 + j * 512 + lane * 8) = (u32x2){gq[j][0], gq[j][2]};
      LDSV(L0 + L_RDY + hw * 4) = 1;
    }
    if (hw < 2) {
      const char* kvsrc = ws + OFF_KV;
      const unsigned rb = L0 + L_RING + hw * 24576, rdy = L0 + L_RDY + hw * 4;
      int have = 2;
      auto look = [&](int n) {
        const u32x2 f = LD64(OFF_FKV + ((((T & ~3) + 2 * (n + (lane >> 3)) + hw) & 31) * 8 + (lane & 7)) * 8);
        const u64 miss = ~__ballot(f[0] == tlo && f[1] == thi);
        return miss ? (int)(__builtin_ctzll(miss) >> 3) : 8;
      };
      auto kvdma = [&](int n) {
        const int tt = ((T & ~3) + 2 * n + hw) & 31;
        if (n >= have) {
          have = n + look(n);
          if (n >= have) {
            u32x2 f;
            SPIN((f = LD64(OFF_FKV + (tt * 8 + (lane & 7)) * 8), __all(f[0] == tlo && f[1] == thi)), 4);
            have = n + 1;
          }
        }
#pragma unroll
        for (int j = 0; j < 6; ++j) GLDS(kvsrc + tt * 6144 + j * 1024, lo16b, rb + (n & 3) * 6144 + j * 1024, " sc1");
      };
      have = 2 + look(2);
      kvdma(2); kvdma(3);
      SPIN(RFL(LDSV(L0 + L_RDY + 8 + hw * 4)) != 0u, 1);
      LDSV(rdy) = 2;
      VWAIT(6); LDSV(rdy) = 3;
      VWAIT(0); LDSV(rdy) = 4;
      SPIN(min4(L0 + L_FRE + 16) >= 1u, 2);
#pragma unroll 1
      for (int n = 4; n < 16; ++n) {
        SPIN(min4(L0 + L_FRE + hw * 16) >= (unsigned)(n - 2), 4);
        kvdma(n); VWAIT(0); LDSV(rdy) = n + 1;
        __builtin_amdgcn_s_sleep(8);
      }
      return;
    }
    const int aw = hw - 2;
    const bool ffn = (b >> 3) < 27;
    const int cg = (b & 7) * 27 + (b >> 3);
#pragma unroll
    for (int j = 0; j < 5; ++j) {
      const int it = aw * 320 + j * 64 + lane, blk = it / 160;
      ST128(((u32x4){SENT, SENT, SENT, SENT}), OFF_O + (T * 4 + blk) * 20480 + g * 2560 + (it - blk * 160) * 16);
    }
    SPIN(min(min4(L0 + L_FRE), min4(L0 + L_FRE + 16)) >= 1u, 4);
    VWAIT(0);
    if (lane == 0) ST64(((u32x2){tlo, thi}), OFF_FS + ((T * 8 + g) * 2 + aw) * 8);
    if (ffn) {
      const char* gsrc = ((lane & 1) ? wu : wg) + (size_t)(aw * 320 + perm16(lane >> 1)) * (FF * 2) + cg * 16;
#pragma unroll
      for (int j = 0; j < 10; ++j) { glds16(gsrc + (size_t)j * 32 * (FF * 2), L0 + L_FG + (aw * 10 + j) * 1024); __builtin_amdgcn_s_sleep(14); }
#pragma unroll
      for (int j = 0; j < 5; ++j) GLDS(wd + (size_t)cg * 10240 + (aw * 5 + j) * 1024, lo16b, L0 + L_FD + (aw * 5 + j) * 1024, "");
    } else SPIN(RFL(LDSV(L0 + L_RDY + aw * 4)) >= 8u, 16);
    SPIN(kvready() == 0x5555555555555555ull, 16);
    if (b == 0 && aw == 0 && lane == 0) ST64(((u32x2){tlo, thi}), OFF_EP);
    unsigned key = max(LD32(OFF_KEY + lane * 4), LD32(OFF_KEY + 256 + lane * 4));
    key = max(key, (unsigned)__builtin_amdgcn_update_dpp(0, (int)key, 0x128, 0xf, 0xf, false));
    key = max(key, (unsigned)__builtin_amdgcn_update_dpp(0, (int)key, 0x124, 0xf, 0xf, false));
    key = max(key, (unsigned)__builtin_amdgcn_update_dpp(0, (int)key, 0x122, 0xf, 0xf, false));
    key = max(key, (unsigned)__builtin_amdgcn_update_dpp(0, (int)key, 0x121, 0xf, 0xf, false));
    key = max(max(__builtin_amdgcn_readlane(key, 0), __builtin_amdgcn_readlane(key, 16)),
              max(__builtin_amdgcn_readlane(key, 32), __builtin_amdgcn_readlane(key, 48)));
    const int idx = SEQ - 1 - (int)(key & (SEQ - 1));
    if (aw == 0 && lane == 0) { LDSV(L0 + L_IDX) = (unsigned)idx; LDSV(L0 + L_IDX + 4) = 2; }
    const bool sel = (idx >> 6) == T;
    if (ffn) {
      const u32x2 stu = LD64(OFF_ST + idx * 8);
      const u32x4 xs = *(const u32x4*)(x + (size_t)idx * 1280 + aw * 640 + min(lane, 39) * 16);
      const float m = __uint_as_float(stu[0]), ri = __builtin_amdgcn_rcpf(__uint_as_float(stu[1]));
      u32x4 xn;
#pragma unroll
      for (int d = 0; d < 4; ++d) xn[d] = pk2(rbf(lo16(xs[d]) - m) * ri, rbf(hi16(xs[d]) - m) * ri);
      if (lane < 40) LDSW(u32x4, L0 + L_XS + aw * 640 + lane * 16) = xn;
    }
    if (ffn) {
      VWAIT(0);
      f32x4 a = z4;
#pragma unroll
      for (int s = 0; s < 10; ++s) {
        const unsigned ad = L0 + L_FG + (aw * 320 + s * 32 + rowp) * 32 + trc;
        a = MFMA16(cat8(trread(ad), trread(ad + 256)), LDSR(bf16x8, L0 + L_XS + aw * 640 + s * 64 + g4 * 16), a);
      }
      if (i == 0) LDSW(f32x4, L0 + L_FP + aw * 64 + g4 * 16) = a;
      LGKM0(); LDS_INC(L0 + L_HC);
      SPIN(RFL(LDSV(L0 + L_HC)) >= 2u, 1);
      u32x4 hf = (u32x4){0u, 0u, 0u, 0u};
      {
        const int c8 = lane & 7;
        const float gp = rbf(LDSR(float, L0 + L_FP + c8 * 4) + LDSR(float, L0 + L_FP + 64 + c8 * 4));
        const float up = rbf(LDSR(float, L0 + L_FP + 32 + c8 * 4) + LDSR(float, L0 + L_FP + 96 + c8 * 4));
        const unsigned hb = __float_as_uint(rbf(rbf(gp * sigm(gp)) * up)) >> 16;
#pragma unroll
        for (int d = 0; d < 4; ++d) {
          const unsigned v = __builtin_amdgcn_readlane(hb, 2 * d) | (__builtin_amdgcn_readlane(hb, 2 * d + 1) << 16);
          hf[d] = g4 == 0 ? v : 0u;
        }
      }
      const unsigned da = L0 + L_FD + (i >> 2) * 1280 + aw * 640 + trc;
      float y[5];
#pragma unroll
      for (int n = 0; n < 20; ++n) {
        const f32x4 d = MFMA16(BC8(hf), cat8(trread(da + n * 32), trread(da + n * 32 + 5120)), z4);
        if ((n & 3) == 0) y[n >> 2] = d[0]; else y[n >> 2] = (g4 == (n & 3)) ? d[0] : y[n >> 2];
      }
#pragma unroll
      for (int j = 0; j < 5; ++j) ST32(__float_as_uint(y[j]), OFF_Y + (cg * DM + aw * 320 + j * 64 + lane) * 4);
      VWAIT(0);
      LDS_INC(L0 + L_HC + 4);
      SPIN(RFL(LDSV(L0 + L_HC + 4)) >= 2u, 1);
      if (aw == 0 && lane == 0) ST64(((u32x2){tlo, thi}), OFF_FY + cg * 8);
    }
#pragma unroll 1
    for (int c = 0; c < 10; ++c) { wdma(wo, c >> 1, L_WO + (c >> 1) * 20480, aw * 2 + (c & 1)); __builtin_amdgcn_s_sleep(10); }
    if (sel) {
      u32x4 f0, f1;
      SPIN((f0 = LD128(OFF_FY + lane * 16), f1 = LD128(OFF_FY + min(64 + lane, NYP / 2 - 1) * 16),
            __all(f0[0] == tlo && f0[1] == thi && f0[2] == tlo && f0[3] == thi && f1[0] == tlo && f1[1] == thi && f1[2] == tlo && f1[3] == thi)), 4);
      const int rg = aw * 3 + min(lane / 20, 2), c4 = lane % 20;
      f32x4 ys = z4;
#pragma unroll 1
      for (int j0 = 0; j0 < 36; j0 += 18) {
        u32x4 yv[18];
#pragma unroll
        for (int j = 0; j < 18; ++j) yv[j] = LD128(OFF_Y + ((rg * 36 + j0 + j) * DM + 80 * g + 4 * c4) * 4);
        SB();
#pragma unroll
        for (int j = 0; j < 18; ++j) ys += __builtin_bit_cast(f32x4, yv[j]);
      }
      if (lane < 60) LDSW(f32x4, L0 + L_YR + rg * 320 + c4 * 16) = ys;
      LGKM0(); LDS_INC(L0 + L_HC + 8);
      SPIN(RFL(LDSV(L0 + L_HC + 8)) >= 2u, 1);
      if (lane < 40) {
        const int c = aw * 40 + lane;
        float y = 0.f;
#pragma unroll
        for (int j = 0; j < 6; ++j) y += LDSR(float, L0 + L_YR + j * 320 + c * 4);
        unsigned u = key >> 16; u = (u & 0x8000u) ? (u & 0x7fffu) : (~u & 0xffffu);
        LDSW(float, L0 + L_Y2 + c * 4) = rbf(rbf(y) * rbf(sigm(bf2f((unsigned short)u))));
      }
    }
    {
      u32x2 f;
      SPIN((f = LD64(OFF_FS + (T * 16 + (lane & 15)) * 8), __all(f[0] == tlo && f[1] == thi)), 4);
    }
    VWAIT(0);
    BAR();
    return;
  }

  const bf16x8 ones8 = BC8(((u32x4){ONES2, ONES2, ONES2, ONES2}));
  const int tb = w & 3, ch = w >> 2;
  const bool kvw = (tb == tbk);
  const unsigned L_QF = L_XA + tbk * 20480;
  unsigned sink = 0;
  if (w >= 4) {
    u64 pc;
    asm volatile("s_getpc_b64 %0" : "=s"(pc));
    const char* tp = (const char*)(pc & ~(u64)255) + (((b >> 3) & 7) * 4 + (w - 4)) * (CODE_BYTES / 32) + min(lane, CODE_BYTES / 4096 - 1) * 128;
    asm volatile("global_load_dword %0, %1, off" : "=v"(sink) : "v"(tp) : "memory");
  }
  u32x2 ep = (u32x2){0u, 0u};
  if (kvw && ch == 1) { ep = LD64(OFF_EP); __builtin_amdgcn_s_setprio(2); }
  if (tid < 22) LDSW(unsigned, L0 + L_RDY + tid * 4) = 0u;
  if (tid >= 64 && tid < 76) LDSW(unsigned, L0 + L_FRE + (tid - 64) * 4) = 0u;
  if (tid >= 128 && tid < 208) LDSW(u32x4, L0 + L_ONE + (tid - 128) * 16) = (u32x4){ONES2, ONES2, ONES2, ONES2};
  auto wait_prg = [&](unsigned n) {
    SPIN(min4(L0 + L_PRG) >= n, 1);
  };
  u32x4 xf[20];
  unsigned xo[4], xk[4];
#pragma unroll
  for (int j = 0; j < 4; ++j) {
    const unsigned o = L_XA + i * 256 + (((j * 4 + g4) ^ i) << 4);
    xo[j] = o + tb * 20480; xk[j] = o + tbk * 20480; PIN(xo[j]); PIN(xk[j]);
  }
#define XFRAG(base, s) LDSR(u32x4, base[(s) & 3] + ((s) >> 2) * 4096)
  unsigned kvad = L0 + L_XB + (ch * 320 + 8 * g4 + (i >> 2)) * 40 + trc;
  unsigned kvar = L0 + (i == 0 ? L_WR : L_ONE) + ch * 640 + g4 * 16;
  unsigned xrd = L0 + L_XRD + w * 4, dgl = ((i >> 2) * 16 + i) * 4;
  PIN(kvad); PIN(kvar); PIN(xrd); PIN(dgl);
  BAR();
  if (tb == ((tbk + 3) & 3)) {
    wait_prg(1);
    f32x4 c0 = z4, c1 = z4, c2 = z4;
    bf16x8 p[10], q[10];
#pragma unroll
    for (int s = 0; s < 10; ++s) p[s] = LDSR(bf16x8, kvar + s * 64);
#pragma unroll
    for (int s = 0; s < 10; ++s) c2 = MFMA16(p[s], ones8, c2);
    PIN(c2);
    wait_prg(2);
    if (ch) {
#pragma unroll
      for (int s = 0; s < 10; ++s) q[s] = BC8(XFRAG(xk, 10 + s));
    } else {
#pragma unroll
      for (int s = 0; s < 10; ++s) q[s] = BC8(XFRAG(xk, s));
    }
    SB();
#pragma unroll
    for (int s = 0; s < 10; ++s) { c0 = MFMA16(p[s], q[s], c0); c1 = MFMA16(q[s], q[s], c1); }
    const float sxx = diag16(c1, i, dgl);
    if (g4 == 0) LDSW(f32x4, L0 + L_STP + (ch * 16 + i) * 16) = (f32x4){c0[1], sxx, c0[0], c2[0]};
    LGKM0();
    LDSV(xrd) = 2;
  }
  unsigned kdst, kso;
  {
    const int tbl = tbk & 1;
    const unsigned img = OFF_KV + (T * 2 + (tbk >> 1)) * 3072;
    if (kt == 0) {
      kdst = lane < 32 ? img + g4 * 512 + (tbl * 16 + i) * 16
           : lane < 48 ? img + 1024 + ((lane >> 3) & 1) * 256 + tbl * 128 + (lane & 7) * 16
           : lane < 56 ? OFF_ST + (T * 64 + tbk * 16) * 8 + (lane & 7) * 16 : 0xfffffff0u;
      kso = L0 + L_KST + (g4 >> 1) * 256 + i * 16 + (g4 & 1) * 8;
    } else {
      kdst = lane < 48 ? img + 1536 + tbl * 768 + lane * 16 : 0xfffffff0u;
      kso = L0 + L_KST + ((g4 & 1) * 24 + i) * 16 + (g4 >> 1) * 8;
    }
    PIN(kdst); PIN(kso);
  }
  if (w >= 4) asm volatile("s_waitcnt vmcnt(0)" : "+v"(sink), "+v"(ep) :: "memory");
  if (kvw && ch == 1) {
    const u64 tag = ((u64)ep[1] << 32 | ep[0]) + 1;
    ep = (u32x2){(unsigned)tag, (unsigned)(tag >> 32)};
    if (lane == 0) { LDSW(u32x2, L0 + L_EP) = ep; LDSV(L0 + L_IDX + 4) = 1; }
  }
  if (!kvw) {
    wait_prg(tb == ((tbk + 1) & 3) ? 3 : 4);
#pragma unroll
    for (int s = 0; s < 20; ++s) xf[s] = XFRAG(xo, s);
    LGKM0();
    LDSV(xrd) = 3;
  } else {
    f32x4 a0 = z4, a1 = z4;
    auto kvloop = [&](auto KT, auto CH) {
      bf16x8 w0[10], w1[10];
      wait_prg(1);
#pragma unroll
      for (int s = 0; s < 10; ++s) {
        const unsigned ad = kvad + s * 1280;
        w0[s] = cat8(trread(ad), trread(ad + 160)); w1[s] = cat8(trread(ad + 32), trread(ad + 192));
      }
      f32x4 c0 = z4, c1 = z4;
#pragma unroll
      for (int s = 0; s < 10; ++s) { c0 = MFMA16(w0[s], ones8, c0); c1 = MFMA16(w1[s], ones8, c1); }
      LDSV(xrd) = 1;
      if (i == 0) { LDSW(f32x4, L0 + L_CSP + ch * 160 + g4 * 16) = c0; LDSW(f32x4, L0 + L_CSP + ch * 160 + 64 + g4 * 16) = c1; }
      wait_prg(2);
#pragma unroll
      for (int s = 0; s < 10; ++s) xf[CH.value * 10 + s] = XFRAG(xo, CH.value * 10 + s);
      SB();
#pragma unroll
      for (int s = 0; s < 10; ++s) {
        const bf16x8 xv = BC8(xf[CH.value * 10 + s]);
        if (KT.value == 0) { a0 = MFMA16(w0[s], xv, a0); a1 = MFMA16(w1[s], xv, a1); }
        else { a0 = MFMA16(xv, w0[s], a0); a1 = MFMA16(xv, w1[s], a1); }
      }
      SB();
#pragma unroll
      for (int s = 0; s < 10; ++s) xf[(1 - CH.value) * 10 + s] = XFRAG(xo, (1 - CH.value) * 10 + s);
    };
    using I0 = std::integral_constant<int, 0>; using I1 = std::integral_constant<int, 1>;
    if (kt == 0) { if (ch) kvloop(I0{}, I1{}); else kvloop(I0{}, I0{}); }
    else { if (ch) kvloop(I1{}, I1{}); else kvloop(I1{}, I0{}); }
    if (ch == 1) { LGKM0(); LDSV(xrd) = 3; }
    if (ch == 0) {
      LDSW(f32x4, L0 + L_EX + lane * 16) = a0; LDSW(f32x4, L0 + L_EX + 1024 + lane * 16) = a1;
      LGKM0();
      LDSV(xrd) = 3;
    } else {
      {
        const unsigned fj = L0 + L_XRD + ((tbk + 3) & 3) * 4;
        SPIN(RFL(min(LDSV(L0 + L_XRD + tbk * 4), min(LDSV(fj), LDSV(fj + 16)))) >= 2u, 0);
      }
      a0 += LDSR(f32x4, L0 + L_EX + lane * 16); a1 += LDSR(f32x4, L0 + L_EX + 1024 + lane * 16);
      auto tstat = [&](int tl) { return LDSR(f32x4, L0 + L_STP + tl * 16) + LDSR(f32x4, L0 + L_STP + (16 + tl) * 16); };
      unsigned key = 0;
      if (kt == 0) {
        const f32x4 st = tstat(i);
        const f32x4 q0 = LDSR(f32x4, L0 + L_CSP + g4 * 16) + LDSR(f32x4, L0 + L_CSP + 160 + g4 * 16);
        const f32x4 q1 = LDSR(f32x4, L0 + L_CSP + 64) + LDSR(f32x4, L0 + L_CSP + 160 + 64);
        float mb, db; ln_stats(st[0], st[1], mb, db);
        if (g4 == 0) LDSW(f32x2, L0 + L_STF + (tb * 16 + i) * 8) = (f32x2){mb, db};
        const float ri = __builtin_amdgcn_rcpf(db);
        const f32x4 v0 = (a0 - mb * q0) * ri, v1 = (a1 - mb * q1) * ri;
        LDSW(u32x2, kso) = (u32x2){pk2(v0[0], v0[1]), pk2(v0[2], v0[3])};
        u32x2 e = (u32x2){pk2(v1[0], v1[1]), pk2(v1[2], v1[3])};
        if (g4 == 1) e = (u32x2){0x00003F80u, 0u};
        if (g4 == 2) e = (u32x2){__float_as_uint(mb), __float_as_uint(db)};
        LDSW(u32x2, L0 + L_KST + 512 + lane * 8) = e;
        unsigned u = __float_as_uint(rbf((st[2] - mb * st[3]) * ri)) >> 16;
        u = (u & 0x8000u) ? (~u & 0xffffu) : (u | 0x8000u);
        key = (u << 16) | (unsigned)(SEQ - 1 - (T * 64 + tbk * 16 + i));
      } else {
        const float q0 = LDSR(float, L0 + L_CSP + i * 4) + LDSR(float, L0 + L_CSP + 160 + i * 4);
        const float q1 = LDSR(float, L0 + L_CSP + 64 + i * 4) + LDSR(float, L0 + L_CSP + 160 + 64 + i * 4);
        const f32x4 st = tstat(i);
        float mb, db; ln_stats(st[0], st[1], mb, db);
        if (g4 == 0) LDSW(f32x2, L0 + L_STF + (tb * 16 + i) * 8) = (f32x2){mb, db};
        const float ri = __builtin_amdgcn_rcpf(db);
        float v0[4], v1[4];
#pragma unroll
        for (int r = 0; r < 4; ++r) {
          const float m = __int_as_float(__builtin_amdgcn_ds_bpermute((4 * g4 + r) * 4, __float_as_int(mb)));
          const float c = __int_as_float(__builtin_amdgcn_ds_bpermute((4 * g4 + r) * 4, __float_as_int(ri)));
          v0[r] = (a0[r] - m * q0) * c; v1[r] = (a1[r] - m * q1) * c;
        }
        LDSW(u32x2, kso) = (u32x2){pk2(v0[0], v0[1]), pk2(v0[2], v0[3])};
        u32x2 e = (u32x2){pk2(v1[0], v1[1]), pk2(v1[2], v1[3])};
        if (i == 4) e = (u32x2){ONES2, ONES2};
        if (i > 4) e = (u32x2){0u, 0u};
        if (i < 8) LDSW(u32x2, kso + 256) = e;
      }
      {
        const u32x4 pv = LDSR(u32x4, L0 + L_KST + lane * 16);
        const unsigned gd = lane < 48 ? OFF_KG + (kdst - OFF_KV) * 2 : 0xffffffe0u;
        ST128(((u32x4){pv[0], ep[0], pv[1], ep[0]}), gd);
        ST128(((u32x4){pv[2], ep[0], pv[3], ep[0]}), gd + 16);
        ST128(pv, kdst);
        LDSV(L0 + L_HC + 12) = 1;
      }
      if (kt == 0) {
        key = max(key, (unsigned)__builtin_amdgcn_update_dpp(0, (int)key, 0x128, 0xf, 0xf, false));
        key = max(key, (unsigned)__builtin_amdgcn_update_dpp(0, (int)key, 0x124, 0xf, 0xf, false));
        key = max(key, (unsigned)__builtin_amdgcn_update_dpp(0, (int)key, 0x122, 0xf, 0xf, false));
        key = max(key, (unsigned)__builtin_amdgcn_update_dpp(0, (int)key, 0x121, 0xf, 0xf, false));
        if (lane == 0) ST32(key, OFF_KEY + (T * 4 + tbk) * 4);
      }
    }
  }
  {
    f32x4 acc[3] = {z4, z4, z4}, cs = z4;
    if (ch == 1 && !kvw) {
      f32x4 s1 = z4, s2 = z4;
#pragma unroll
      for (int k = 0; k < 20; ++k) { s1 = MFMA16(ones8, BC8(xf[k]), s1); s2 = MFMA16(BC8(xf[k]), BC8(xf[k]), s2); }
      float mb, db; ln_stats(s1[0], diag16(s2, i, dgl), mb, db);
      if (g4 == 0) LDSW(f32x2, L0 + L_STF + (tb * 16 + i) * 8) = (f32x2){mb, db};
    }
    const int own = (tb - tbk - 1) & 3;
    auto qblk = [&](int n) { const int nb = 3 - ch, v = min(own, nb - 1) + n; return ch * 3 + (v >= nb ? v - nb : v); };
    unsigned qo[3];
#pragma unroll
    for (int n = 0; n < 3; ++n) {
      const int c4 = 16 * qblk(n) + 4 * g4, hh = c4 / 20, dim = c4 - hh * 20, r = (tb & 1) * 16 + i;
      qo[n] = L0 + L_QF + (hh * 2 + (tb >> 1)) * 1536 + (dim < 16 ? ((dim >> 3) * 32 + r) * 16 + (dim & 4) * 2 : 1024 + r * 8);
      PIN(qo[n]);
    }
    bool pub = !(kvw && ch == 1);
    auto qloop = [&](auto CH) {
      constexpr int NB = 3 - CH.value;
      unsigned lo[3], hi[3];
#pragma unroll
      for (int n = 0; n < NB; ++n) { lo[n] = rowp * 160 + trc + qblk(n) * 32; hi[n] = lo[n] + L_S0; PIN(lo[n]); PIN(hi[n]); }
      bf16x8 wf[3][3];
      u32x4 pw;
      auto rd = [&](int k, bf16x8 (&d)[3]) {
        const int c = k >> 2;
        if (c >= 2 && (k & 3) == 0) {
#pragma unroll
          for (int n = 0; n < NB; ++n) { hi[n] = lo[n] + (c == 2 ? L_XB : L_XA + ((tbk + c - 2) & 3) * 20480); PIN(hi[n]); }
        }
        const unsigned sl = (c == 1 ? 20480 : 0) + (k & 3) * 5120;
#pragma unroll
        for (int n = 0; n < NB; ++n) d[n] = cat8(trread(hi[n] + sl), trread(hi[n] + sl + 1280));
      };
      wait_prg(5);
      rd(0, wf[0]); rd(1, wf[1]);
#pragma unroll
      for (int k = 0; k < 20; ++k) {
        if (k + 2 < 20) {
          if (((k + 2) & 3) == 0) {
            if (RFL(min(min(pw[0], pw[1]), min(pw[2], pw[3]))) < 5u + ((k + 2) >> 2)) wait_prg(5 + ((k + 2) >> 2));
          }
          if ((k & 3) == 1) pw = *(volatile LAS u32x4*)(uintptr_t)(L0 + L_PRG);
          rd(k + 2, wf[(k + 2) % 3]);
        }
        if (k == 6) LDS_INC(L0 + L_CNT);
        if (CH.value == 1 && (k & 3) == 3 && !pub) {
          const unsigned ib = __builtin_amdgcn_s_getreg(7 | (31 << 11));
          if ((ib & 0x00c0000fu) == 0u) { VWAIT(0); if (lane == 0) ST64(ep, OFF_FKV + (T * 8 + g) * 8); pub = true; }
        }
        SB();
        const bf16x8 xq = BC8(xf[k]);
#pragma unroll
        for (int n = 0; n < NB; ++n) acc[n] = MFMA16(wf[k % 3][n], xq, acc[n]);
        cs = MFMA16(wf[k % 3][0], ones8, cs);
        SB();
      }
    };
    if (ch) qloop(std::integral_constant<int, 1>{}); else qloop(std::integral_constant<int, 0>{});
    if (i == 0 && own < (ch ? 2 : 3)) LDSW(f32x4, L0 + L_CSQ + (ch * 3 + own) * 64 + g4 * 16) = cs;
    if (!kvw) LDS_INC(L0 + L_CNT + 8 + ch * 4);
    SPIN(RFL(LDSV(L0 + L_CNT + 8 + ch * 4)) >= 3u, 0);
    SPIN(RFL(LDSV(L0 + L_CNT)) >= 8u, 0);
    const f32x2 st = LDSR(f32x2, L0 + L_STF + (tb * 16 + i) * 8);
    const float f = QSCALE * __builtin_amdgcn_rcpf(st[1]);
#pragma unroll
    for (int n = 0; n < 3; ++n) {
      if (n == 2 && ch == 1) break;
      const f32x4 v = (acc[n] - st[0] * LDSR(f32x4, L0 + L_CSQ + qblk(n) * 64 + g4 * 16)) * f;
      LDSW(u32x2, qo[n]) = (u32x2){pk2(v[0], v[1]), pk2(v[2], v[3])};
    }
    LDS_INC(L0 + L_CNT + 4);
    if (!pub) {
      VWAIT(0);
      if (lane == 0) ST64(ep, OFF_FKV + (T * 8 + g) * 8);
    }
  }
  const int r = lane & 31, h = lane >> 5, qp = w & 3, kh = w >> 2;
  const unsigned koff = lane * 16, xoff = 1024 + lane * 8, voff = 1536 + (h * 24 + min(r, 23)) * 16;
  f32x16 zero16;
#pragma unroll
  for (int n = 0; n < 16; ++n) zero16[n] = 0.f;
  __builtin_amdgcn_s_setprio(0);
  SPIN(RFL(LDSV(L0 + L_CNT + 4)) >= 8u, 0);
  const bf16x8 qf0 = LDSR(bf16x8, L0 + L_QF + qp * 3072 + koff), qf1 = LDSR(bf16x8, L0 + L_QF + qp * 3072 + 1536 + koff);
  u32x2 qx0 = LDSR(u32x2, L0 + L_QF + qp * 3072 + 1024 + r * 8), qx1 = LDSR(u32x2, L0 + L_QF + qp * 3072 + 1536 + 1024 + r * 8);
  if (h) { qx0 = (u32x2){0u, 0u}; qx1 = qx0; }
  SPIN(RFL(LDSV(L0 + L_RDY)) >= 1u, 0);
  {
    const bf16x8 k0 = LDSR(bf16x8, L0 + L_RING + koff); const s16x4 kx = LDSR(s16x4, L0 + L_RING + xoff);
    const f32x16 s0 = MFMA32K8(kx, __builtin_bit_cast(s16x4, qx0), MFMA32(k0, qf0, zero16));
    const f32x16 s1 = MFMA32K8(kx, __builtin_bit_cast(s16x4, qx1), MFMA32(k0, qf1, zero16));
    float m0 = s0[0], m1 = s1[0];
#pragma unroll
    for (int n = 1; n < 16; ++n) { m0 = fmaxf(m0, s0[n]); m1 = fmaxf(m1, s1[n]); }
    { const auto a = __builtin_amdgcn_permlane32_swap(__float_as_uint(m0), __float_as_uint(m0), false, false);
      const auto c = __builtin_amdgcn_permlane32_swap(__float_as_uint(m1), __float_as_uint(m1), false, false);
      m0 = fmaxf(__uint_as_float(a[0]), __uint_as_float(a[1])); m1 = fmaxf(__uint_as_float(c[0]), __uint_as_float(c[1])); }
    if (h) { qx0[0] = pk2(-m0, 0.f); qx1[0] = pk2(-m1, 0.f); }
  }
  const s16x4 qxf0 = __builtin_bit_cast(s16x4, qx0), qxf1 = __builtin_bit_cast(s16x4, qx1);
  const unsigned rdy = L0 + L_RDY + kh * 4, fre = L0 + L_FRE + w * 4;
  const unsigned RB = L0 + L_RING + kh * 24576;
  LDSV(fre) = 1;
  if (kh) SPIN(RFL(LDSV(rdy)) >= 1u, 0);
  unsigned kb = RB + koff, xb = RB + xoff, vb = RB + voff, rv = 0, pv_ = 0;
  const unsigned pfre = L0 + L_FRE + (w ^ 4) * 4;
  PIN(kb); PIN(xb); PIN(vb);
#define TOFF(p, sl) ((sl) * 6144 + (p) * 3072)
#define LDK(p, sl) LDSR(bf16x8, kb + TOFF(p, sl))
#define LDX(p, sl) LDSR(s16x4, xb + TOFF(p, sl))
#define LDV(p, sl, s) LDSR(bf16x8, vb + TOFF(p, sl) + (s) * 768)
  f32x16 o0 = zero16, o1 = zero16;
  bf16x8 kA = LDK(0, 0), kB = LDK(1, 0), v0A = LDV(0, 0, 0), v1A = LDV(0, 0, 1), v0B, v1B;
  s16x4 xA = LDX(0, 0), xB = LDX(1, 0);
  f32x16 sa = MFMA32K8(xA, qxf0, MFMA32(kA, qf0, zero16)), sb;
  u32x4 p1 = (u32x4){0u, 0u, 0u, 0u};
  bf16x8 v1P = v1A;
#define EX(S, n) S[n] = __builtin_amdgcn_exp2f(S[n])
#define EX4(S, n) EX(S, n); EX(S, n + 1); EX(S, n + 2); EX(S, n + 3)
#define PK4(S, n) (u32x4){pk2(S[n], S[n + 1]), pk2(S[n + 2], S[n + 3]), pk2(S[n + 4], S[n + 5]), pk2(S[n + 6], S[n + 7])}
#define STEP(kc, xc, kn, xn, v0c, v1c, v0n, v1n, KP, KS, VP, VS)          \
  {                                                                       \
    sb = MFMA32(kc, qf1, zero16); SB(); EX4(sa, 0); PIN(sa); SB();        \
    o1 = MFMA32(v1P, BC8(p1), o1); SB(); EX4(sa, 4);                      \
    u32x4 p0 = PK4(sa, 0); PIN(p0); SB();                                 \
    v0n = LDV(VP, VS, 0); v1n = LDV(VP, VS, 1); SB();                     \
    sb = MFMA32K8(xc, qxf1, sb); SB(); EX4(sa, 8); PIN(sa); SB();         \
    kc = LDK(KP, KS); xc = LDX(KP, KS); SB();                             \
    o0 = MFMA32(v0c, BC8(p0), o0); SB(); EX4(sa, 12);                     \
    p1 = PK4(sa, 8); PIN(p1); SB();                                       \
    sa = MFMA32(kn, qf0, zero16); SB(); EX4(sb, 0); PIN(sb); SB();        \
    o0 = MFMA32(v1c, BC8(p1), o0); SB(); EX4(sb, 4);                      \
    p0 = PK4(sb, 0); PIN(p0); SB();                                       \
    sa = MFMA32K8(xn, qxf0, sa); SB(); EX4(sb, 8); PIN(sb); SB();         \
    o1 = MFMA32(v0c, BC8(p0), o1); SB(); EX4(sb, 12);                     \
    p1 = PK4(sb, 8); PIN(p1); v1P = v1c; SB();                            \
  }
#define CHUNK(sl, sn)                                                     \
  {                                                                       \
    if (RFL(rv) < n + 2 && n < 15) SPIN(RFL(LDSV(rdy)) >= n + 2, 0);      \
    if (RFL(pv_) + kh > n + 1) __builtin_amdgcn_s_setprio(1); else __builtin_amdgcn_s_setprio(0); \
    STEP(kA, xA, kB, xB, v0A, v1A, v0B, v1B, 0, sn, 1, sl);               \
    LDSV(fre) = n + 2; rv = LDSV(rdy); pv_ = LDSV(pfre); SB();            \
    STEP(kB, xB, kA, xA, v0B, v1B, v0A, v1A, 1, sn, 0, sn);               \
    ++n;                                                                  \
  }
  {
    unsigned n = 0;
#pragma unroll 1
    for (int it = 0; it < 4; ++it) {
      CHUNK(0, 1); CHUNK(1, 2); CHUNK(2, 3); CHUNK(3, 0);
    }
  }
  o1 = MFMA32(v1P, BC8(p1), o1);
  PIN(ep);
  __builtin_amdgcn_s_setprio(0);
  const int tok = T * 64 + tb * 16 + i;
  const unsigned eoff = tok * 1280 + (80 * g + 48 * ch + 4 * g4) * 2;
  u32x2 xe[3];
#pragma unroll
  for (int n = 0; n < 3; ++n) xe[n] = *(const u32x2*)(x + eoff + ((ch == 1 && n == 2) ? 0 : n * 32));
  SPIN(min4(L0 + L_FRE + kh * 16) >= 15u, 2);
  {
    const f32x16 os = kh ? o0 : o1;
    const unsigned e = RB + qp * 3072 + lane * 16;
    LDSW(f32x4, e) = (f32x4){os[0], os[1], os[2], os[3]};
    LDSW(f32x4, e + 1024) = (f32x4){os[4], os[5], os[6], os[7]};
    LDSW(f32x4, e + 2048) = (f32x4){os[8], os[9], os[10], os[11]};
    LGKM0();
    LDSV(xrd) = 7;
  }
  SPIN(RFL(LDSV(L0 + L_XRD + (w ^ 4) * 4)) == 7u, 2);
  f32x16 o = kh ? o1 : o0;
  const unsigned pe = L0 + L_RING + (kh ^ 1) * 24576 + qp * 3072;
  {
    const f32x4 a = LDSR(f32x4, pe + lane * 16), c = LDSR(f32x4, pe + 1024 + lane * 16), d = LDSR(f32x4, pe + 2048 + lane * 16);
#pragma unroll
    for (int n = 0; n < 4; ++n) { o[n] += a[n]; o[4 + n] += c[n]; o[8 + n] += d[n]; }
  }
  float l = __uint_as_float(__builtin_amdgcn_permlane32_swap(__float_as_uint(o[8]), __float_as_uint(o[8]), false, false)[1]);
  {
    bool ok = (l >= 8.6736174e-19f) && (l <= 1.1529215e18f);
#pragma unroll
    for (int n = 0; n < 12; ++n) ok = ok && (fabsf(o[n]) <= 3.0e38f);
    if (!__all(ok)) {
      SPIN(RFL(LDSV(L0 + L_IDX + 4)) >= 2u, 4);
      const bf16x8 qf = kh ? qf1 : qf0;
      const u32x2 qq = kh ? qx1 : qx0;
      const s16x4 qz = __builtin_bit_cast(s16x4, h ? (u32x2){0u, 0u} : qq);
      float m = -INFINITY;
#pragma unroll 1
      for (int t = 0; t < 64; ++t) {
        f32x16 s = MFMA32(BC8(LD128(OFF_KV + t * 3072 + koff)), qf, zero16);
        s = MFMA32K8(__builtin_bit_cast(s16x4, LD64(OFF_KV + t * 3072 + xoff)), qz, s);
#pragma unroll
        for (int n = 0; n < 16; ++n) m = fmaxf(m, s[n]);
      }
      m = fmaxf(m, __shfl_xor(m, 32));
      o = zero16;
#pragma unroll 1
      for (int t = 0; t < 64; ++t) {
        f32x16 s = MFMA32(BC8(LD128(OFF_KV + t * 3072 + koff)), qf, zero16);
        s = MFMA32K8(__builtin_bit_cast(s16x4, LD64(OFF_KV + t * 3072 + xoff)), qz, s);
#pragma unroll
        for (int n = 0; n < 16; ++n) s[n] = __builtin_amdgcn_exp2f(s[n] - m);
        o = MFMA32(BC8(LD128(OFF_KV + t * 3072 + voff)), BC8(PK4(s, 0)), o);
        o = MFMA32(BC8(LD128(OFF_KV + t * 3072 + 768 + voff)), BC8(PK4(s, 8)), o);
      }
      l = __shfl(o[8], r + 32);
    }
  }
  {
    const float li = __builtin_amdgcn_rcpf(l);
#pragma unroll
    for (int n = 0; n < 3; ++n) {
      const u32x2 v = (u32x2){pk2(o[4 * n] * li, o[4 * n + 1] * li), pk2(o[4 * n + 2] * li, o[4 * n + 3] * li)};
      if (n < 2 || !h) LDSW(u32x2, pe + (2 * n + h) * 256 + r * 8) = v;
    }
    BAR();
    const unsigned odst = OFF_O + (T * 4 + (w >> 1)) * 20480 + g * 2560 + (w & 1) * 1280 + koff;
    const int blk = w >> 1;
    const unsigned sbase = L0 + L_RING + ((blk >> 1) ^ 1) * 24576 + ((blk & 1) * 16 + i) * 8;
#pragma unroll
    for (int k = 0; k < 2; ++k) {
      const int c8 = (w & 1) * 5 + (k ? 4 : g4);
      const int d0 = 2 * c8, d1 = 2 * c8 + 1;
      const u32x2 lo = LDSR(u32x2, sbase + (d0 / 5) * 3072 + (d0 % 5) * 256), hi = LDSR(u32x2, sbase + (d1 / 5) * 3072 + (d1 % 5) * 256);
      if (k == 0 || lane < 16) ST128(((u32x4){lo[0], lo[1], hi[0], hi[1]}), odst + k * 1024);
    }
    LGKM0();
    LDSV(xrd) = 11;
  }
  u32x2 gt[3];
  {
    const bool routed = ((int)LDSV(L0 + L_IDX) == tok);
    const f32x2 st = LDSR(f32x2, L0 + L_STF + (tb * 16 + i) * 8);
    const float ri = __builtin_amdgcn_rcpf(st[1]);
#pragma unroll
    for (int n = 0; n < 3; ++n) {
      const float xv[4] = {lo16(xe[n][0]), hi16(xe[n][0]), lo16(xe[n][1]), hi16(xe[n][1])};
      float z[4];
#pragma unroll
      for (int e = 0; e < 4; ++e) z[e] = rbf(rbf(xv[e] - st[0]) * ri);
      if (routed) {
        const f32x4 y = LDSR(f32x4, L0 + L_Y2 + (min(kh * 3 + n, 4) * 16 + 4 * g4) * 4);
#pragma unroll
        for (int e = 0; e < 4; ++e) z[e] = rbf(z[e] + y[e]);
      }
      gt[n] = (u32x2){pk2(z[0] * sigm(z[0]), z[1] * sigm(z[1])), pk2(z[2] * sigm(z[2]), z[3] * sigm(z[3]))};
      PIN(gt[n]);
    }
  }
  f32x4 acc[5] = {z4, z4, z4, z4, z4};
  {
    const unsigned wb = L0 + L_WO + (kh * 320 + rowp) * 160 + trc;
    bf16x8 wf[4][5];
#define WOF(s, n) cat8(trread(wb + (s) * 5120 + (n) * 32), trread(wb + (s) * 5120 + (n) * 32 + 1280))
#pragma unroll
    for (int s = 0; s < 4; ++s)
#pragma unroll
      for (int n = 0; n < 5; ++n) { wf[s][n] = WOF(s, n); PIN(wf[s][n]); }
    u32x4 of[10];
    const unsigned osrc = OFF_O + ((T * 4 + tb) * 20 + kh * 10) * 1024 + lane * 16;
    _Pragma("nounroll") for (unsigned sp_ = 0; sp_ < SPIN_CAP; ++sp_) {
#pragma unroll
      for (int s = 0; s < 10; ++s) of[s] = LD128(osrc + s * 1024);
      SB();
      unsigned mx = 0;
#pragma unroll
      for (int s = 0; s < 10; ++s) mx = max(max(mx, max(of[s][0], of[s][1])), max(of[s][2], of[s][3]));
      if (!__any(mx == SENT)) break;
    }
#pragma unroll
    for (int s = 0; s < 10; ++s) {
#pragma unroll
      for (int n = 0; n < 5; ++n) {
        acc[n] = MFMA16(wf[s & 3][n], BC8(of[s]), acc[n]);
        if (s + 4 < 10) wf[s & 3][n] = WOF(s + 4, n);
      }
      SB();
    }
  }
  f32x4 own[3];
  SPIN(min(min4(L0 + L_XRD), min4(L0 + L_XRD + 16)) >= 11u, 1);
  {
    const unsigned e = L0 + L_RING + w * 3072 + lane * 16;
    if (kh == 0) { LDSW(f32x4, e) = acc[3]; LDSW(f32x4, e + 1024) = acc[4]; own[0] = acc[0]; own[1] = acc[1]; }
    else { LDSW(f32x4, e) = acc[0]; LDSW(f32x4, e + 1024) = acc[1]; LDSW(f32x4, e + 2048) = acc[2]; own[0] = acc[3]; own[1] = acc[4]; }
    own[2] = acc[2];
    LGKM0();
    LDSV(xrd) = 15;
  }
  unsigned eo2 = lane; PIN(eo2); eo2 = (T * 64 + tb * 16 + (eo2 & 15)) * 1280 + (80 * g + 48 * kh + 4 * (eo2 >> 4)) * 2;
  SPIN(RFL(LDSV(L0 + L_XRD + (w ^ 4) * 4)) == 15u, 1);
#pragma unroll
  for (int n = 0; n < 3; ++n) {
    if (kh == 1 && n == 2) break;
    const f32x4 s = own[n] + LDSR(f32x4, L0 + L_RING + (w ^ 4) * 3072 + n * 1024 + lane * 16);
    PIN(xe[n]);
    const float xv[4] = {lo16(xe[n][0]), hi16(xe[n][0]), lo16(xe[n][1]), hi16(xe[n][1])};
    const float gv[4] = {lo16(gt[n][0]), hi16(gt[n][0]), lo16(gt[n][1]), hi16(gt[n][1])};
    float ov[4];
#pragma unroll
    for (int e = 0; e < 4; ++e) ov[e] = xv[e] + rbf(rbf(s[e]) * gv[e]);
    *(u32x2*)(out + eo2 + n * 32) = (u32x2){pk2(ov[0], ov[1]), pk2(ov[2], ov[3])};
  }
}

extern "C" void kernel_launch(void* const* d_in, const int* in_sizes, int n_in, void* d_out, int out_size, void* d_ws,
                              size_t ws_size, hipStream_t stream) {
  (void)hipFuncSetAttribute((const void*)fused_block, hipFuncAttributeMaxDynamicSharedMemorySize, LDS_BYTES);
  fused_block<<<256, 768, LDS_BYTES, stream>>>((const char*)d_in[0], (const char*)d_in[1], (const char*)d_in[2], (const char*)d_in[3],
                                               (const char*)d_in[4], (const char*)d_in[6], (const char*)d_in[7], (const char*)d_in[8],
                                               (const char*)d_in[9], (char*)d_ws, (char*)d_out);
}
```
